```python
import torch

def _softmax(x, axis=-1):
    return torch.softmax(x, dim=axis)

def _gelu(x):
    return torch.nn.functional.gelu(x, approximate='tanh')

def _silu(x):
    return torch.nn.functional.silu(x)

def _sigmoid(x):
    return torch.sigmoid(x)

def _layernorm(x, axis=-1, eps=1e-05):
    m = x.mean(dim=axis, keepdim=True)
    v = x.var(dim=axis, keepdim=True, unbiased=False)
    return (x - m) / torch.sqrt(v + eps)

def _scatter_add(tgt, idx, src, axis=0):
    return tgt.clone().index_add_(axis, idx, src)

def _segment_sum(src, seg, n):
    out = torch.zeros((n,) + src.shape[1:], dtype=src.dtype)
    return out.index_add_(0, seg, src)

def _pad(x, pw):
    flat = tuple((v for pair in reversed(pw) for v in pair))
    return torch.nn.functional.pad(x, flat)
kernel_name = 'concat_heads__memory_aug_recurrent_mem_tr__mha_block__prompt'

def setup_inputs():

    _w = lambda a: (lambda t: t.to(torch.bfloat16) if t.is_floating_point() else t)(torch.as_tensor(a))

    def _A_setup():
        B, S, Dm = (1, 2048, 512)
        H, Dh = (32, 128)
        M, seg = (8, 32)
        seg = min(seg, S)
        S = S // seg * seg
        return {'x': _w((lambda *s: torch.randn(*s, dtype=torch.bfloat16) * 0.05)(B, S, Dm)), 'mem0': _w((lambda *s: torch.randn(*s, dtype=torch.bfloat16) * 0.8)(B, M, Dm)), 'Wq': _w((lambda *s: torch.randn(*s, dtype=torch.bfloat16) * 1.6)(Dm, H, Dh) * Dm ** (-0.5)), 'Wk': _w((lambda *s: torch.randn(*s, dtype=torch.bfloat16) * 1.6)(Dm, H, Dh) * Dm ** (-0.5)), 'Wv': _w((lambda *s: torch.randn(*s, dtype=torch.bfloat16) * 0.8)(Dm, H, Dh) * Dm ** (-0.5)), 'Wo': _w((lambda *s: torch.randn(*s, dtype=torch.bfloat16) * 0.8)(H, Dh, Dm) * (H * Dh) ** (-0.5)), 'seg_len': seg}
    _A_in = _A_setup()

    def _B_setup():
        B, S, D, H = (1, 2048, 512, 32)
        return {'x': _w((lambda *s: torch.randn(*s, dtype=torch.bfloat16) * 0.2)(B, S, D)), 'wqkv': _w((lambda *s: torch.randn(*s, dtype=torch.bfloat16) * 0.2)(D, 3 * D)), 'wo': _w((lambda *s: torch.randn(*s, dtype=torch.bfloat16) * 0.05)(D, D))}
    _B_in = _B_setup()
    return {'A_x': _A_in['x'], 'A_mem0': _A_in['mem0'], 'A_Wq': _A_in['Wq'], 'A_Wk': _A_in['Wk'], 'A_Wv': _A_in['Wv'], 'A_Wo': _A_in['Wo'], 'A_seg_len': _A_in['seg_len'], 'B_x': _B_in['x'], 'B_wqkv': _B_in['wqkv'], 'B_wo': _B_in['wo']}

def reference(A_x, A_mem0, A_Wq, A_Wk, A_Wv, A_Wo, A_seg_len, B_x, B_wqkv, B_wo):

    def _A_ref(x, mem0, Wq, Wk, Wv, Wo, seg_len):
        B, S, Dm = x.shape
        H, Dh = (Wq.shape[1], Wq.shape[2])
        M = mem0.shape[1]
        scale = Dh ** (-0.5)
        n_seg = S // seg_len
        L = M + seg_len
        idx = torch.arange(L)
        mask = (idx[:, None] < M) | (idx[:, None] >= idx[None, :])
        mem = mem0
        outs = []
        for s in range(n_seg):
            xs = x[:, s * seg_len:(s + 1) * seg_len, :].clone()
            h = torch.concatenate([mem, xs], axis=1)
            q = torch.einsum('btd,dhe->bhte', h, Wq)
            k = torch.einsum('btd,dhe->bhte', h, Wk)
            v = torch.einsum('btd,dhe->bhte', h, Wv)
            scores = torch.einsum('bhqe,bhke->bhqk', q, k) * scale
            scores = torch.where(mask[None, None], scores, -1000000000.0)
            p = _softmax(scores, axis=-1)
            o = torch.einsum('bhqk,bhke->bhqe', p, v)
            o = torch.einsum('bhte,hed->btd', o, Wo)
            mem = o[:, :M, :].clone()
            outs.append(o[:, M:, :])
        return torch.concatenate(outs, axis=1)

    def _B_ref(x, wqkv, wo):
        B, S, D = x.shape
        H = 32
        Dh = D // H
        qkv = (lambda x, *d: x.permute(*d))((x @ wqkv).reshape(B, S, 3, H, Dh), 2, 0, 3, 1, 4)
        q, k, v = (qkv[0], qkv[1], qkv[2])
        scores = torch.einsum('bhqd,bhkd->bhqk', q, k) * Dh ** (-0.5)
        p = _softmax(scores, axis=-1)
        o = torch.einsum('bhqk,bhkd->bhqd', p, v)
        return (lambda x, *d: x.permute(*d))(o, 0, 2, 1, 3).reshape(B, S, D) @ wo
    a = _A_ref(A_x, A_mem0, A_Wq, A_Wk, A_Wv, A_Wo, A_seg_len)
    b = _B_ref(B_x, B_wqkv, B_wo)
    return torch.concatenate([a, b], axis=-1)
```

```cpp
#include <hip/hip_runtime.h>
#include <hip/hip_bf16.h>
#include <stdint.h>

typedef unsigned short u16;
typedef __bf16 bf16x2 __attribute__((ext_vector_type(2)));
typedef __bf16 bf16x4 __attribute__((ext_vector_type(4)));
typedef __bf16 bf16x8 __attribute__((ext_vector_type(8)));
typedef float f32x2 __attribute__((ext_vector_type(2)));
typedef float f32x4 __attribute__((ext_vector_type(4)));
typedef float f32x16 __attribute__((ext_vector_type(16)));
typedef unsigned u32x2 __attribute__((ext_vector_type(2)));
typedef unsigned u32x4 __attribute__((ext_vector_type(4)));
typedef unsigned u32x16 __attribute__((ext_vector_type(16)));
#define LAS __attribute__((address_space(3)))
#define GAS __attribute__((address_space(1)))
#define DEVI __device__ __forceinline__

constexpr int SEQ = 2048, DM = 512, NSEG = 64;
constexpr size_t PAGE = 2u << 20;
constexpr size_t C_END = 19 * PAGE;
constexpr size_t C_CTL = 2 * PAGE + 124 * 16384;
constexpr int CTL_EPOCH = 0, CTL_ROT = 13312, CTL_TMO = 64, CTL_RDY = 128;
constexpr int CTL_PG = 384, NPAGE = 84;
constexpr int CTL_E2 = 1088, CTL_E1 = 1536, CTL_ATB = 2560;
constexpr int CTL_SEG = 4096, CTL_ATA = 8192;
constexpr int CTL_STA = 12288;
constexpr size_t W_KX = C_END + PAGE, W_VX = W_KX + (32u << 20), W_KM = W_VX + (32u << 20), W_O = W_KM + (32u << 20),
                 W_QKVB = W_O + (32u << 20), W_OB = W_QKVB + (6u << 20), W_STAT = W_OB + (2u << 20), W_END = W_STAT + (1u << 20);
DEVI int far_off(int seg, int blk32, unsigned far16) { return (seg * 32 + blk32) * 16384 + (((far16 >> (seg >> 2)) & 1) << 13); }
struct Halves { unsigned k, v, m, o; int c; };
constexpr int LDS_BYTES = 163840;
constexpr int AUX_SC1 = 16;
constexpr unsigned SPIN_CAP = 1u << 16;

#define HDELAY 0
#define HDEPTH 4
struct Params {
  const u16 *ax, *amem0, *awq, *awk, *awv, *awo, *bx, *bwqkv, *bwo;
  u16* out;
  char* ws;
  int dB, dA;
};

#define WG_END() do { __builtin_amdgcn_endpgm(); __builtin_unreachable(); } while (0)
DEVI Params args() {
  const __attribute__((address_space(4))) Params* k = (const __attribute__((address_space(4))) Params*)__builtin_amdgcn_kernarg_segment_ptr();
  asm volatile("" : "+s"(k));
  Params p;
  p.ax = k->ax; p.amem0 = k->amem0; p.awq = k->awq; p.awk = k->awk; p.awv = k->awv; p.awo = k->awo; p.bx = k->bx; p.bwqkv = k->bwqkv; p.bwo = k->bwo;
  p.out = k->out; p.ws = k->ws; p.dB = k->dB; p.dA = k->dA;
  return p;
}
typedef __amdgpu_buffer_rsrc_t rsrc_t;
DEVI rsrc_t mk_rs(const void* base, unsigned bytes) { return __builtin_amdgcn_make_buffer_rsrc((void*)base, 0, (int)bytes, 0x00020000); }
DEVI u32x4 mk_rsrc(const void* base, unsigned bytes) { const uint64_t a = (uint64_t)base; return (u32x4){(unsigned)a, (unsigned)(a >> 32) & 0xffffu, bytes, 0x00020000u}; }
DEVI unsigned pk2(float a, float b) { f32x2 x = {a, b}; return __builtin_bit_cast(unsigned, __builtin_convertvector(x, bf16x2)); }
DEVI int lane_id() { int l; asm volatile("v_mbcnt_lo_u32_b32 %0, -1, 0\n\tv_mbcnt_hi_u32_b32 %0, -1, %0" : "=v"(l)); return l; }
DEVI void lds_barrier() { asm volatile("s_waitcnt lgkmcnt(0)\n\ts_barrier" ::: "memory"); }
DEVI bf16x4 trd(const LAS char* a) { return __builtin_amdgcn_ds_read_tr16_b64_v4bf16((LAS bf16x4*)a); }
DEVI bf16x8 cat8(bf16x4 a, bf16x4 b) { return __builtin_shufflevector(a, b, 0, 1, 2, 3, 4, 5, 6, 7); }
DEVI bf16x8 as_bf8(u32x4 v) { return __builtin_bit_cast(bf16x8, v); }
DEVI u32x4 ld16(rsrc_t r, int voff, int soff) { return __builtin_amdgcn_raw_buffer_load_b128(r, voff, soff, AUX_SC1); }
DEVI void st16(u32x4 v, rsrc_t r, int voff, int soff) { __builtin_amdgcn_raw_buffer_store_b128(v, r, voff, soff, AUX_SC1); }
DEVI void st8(u32x2 v, rsrc_t r, int voff, int soff) { __builtin_amdgcn_raw_buffer_store_b64(v, r, voff, soff, AUX_SC1); }
DEVI float swap16(float v, float& other) {
  unsigned a = __float_as_uint(v), b = a; asm volatile("" : "+v"(b));
  auto r = __builtin_amdgcn_permlane16_swap(a, b, false, false); other = __uint_as_float(r[1]); return __uint_as_float(r[0]);
}
DEVI float swap32(float v, float& other) {
  unsigned a = __float_as_uint(v), b = a; asm volatile("" : "+v"(b));
  auto r = __builtin_amdgcn_permlane32_swap(a, b, false, false); other = __uint_as_float(r[1]); return __uint_as_float(r[0]);
}
#define MF32(a, b, c) __builtin_amdgcn_mfma_f32_16x16x32_bf16((a), (b), (c), 0, 0, 0)
#define MFBIG(a, b, c) __builtin_amdgcn_mfma_f32_32x32x16_bf16((a), (b), (c), 0, 0, 0)

template <int SC1, int OFF = 0>
DEVI void dma16(const rsrc_t r, int voff, int soff, unsigned ldsaddr) {
  if constexpr (SC1 == 2)
    asm volatile("s_mov_b32 m0, %3\n\ts_nop 0\n\tbuffer_load_dwordx4 %0, %1, %2 offen offset:%4 nt lds" :: "v"(voff), "s"(r), "s"(soff), "s"(ldsaddr), "i"(OFF) : "memory");
  else if constexpr (SC1 == 1)
    asm volatile("s_mov_b32 m0, %3\n\ts_nop 0\n\tbuffer_load_dwordx4 %0, %1, %2 offen offset:%4 sc1 lds" :: "v"(voff), "s"(r), "s"(soff), "s"(ldsaddr), "i"(OFF) : "memory");
  else
    asm volatile("s_mov_b32 m0, %3\n\ts_nop 0\n\tbuffer_load_dwordx4 %0, %1, %2 offen offset:%4 lds" :: "v"(voff), "s"(r), "s"(soff), "s"(ldsaddr), "i"(OFF) : "memory");
}
template <int SC1>
DEVI void dma16n(int n, const rsrc_t r, int voff, int soff, unsigned ldsaddr) {
  const int hi = (n >> 2) * 4096;
  if ((n & 3) == 0) dma16<SC1, 0>(r, voff, soff + hi, ldsaddr + hi);
  else if ((n & 3) == 1) dma16<SC1, 1024>(r, voff, soff + hi, ldsaddr + hi);
  else if ((n & 3) == 2) dma16<SC1, 2048>(r, voff, soff + hi, ldsaddr + hi);
  else dma16<SC1, 3072>(r, voff, soff + hi, ldsaddr + hi);
}

struct Ctl { int* w; unsigned ep; };
DEVI int ctl_ld(const Ctl& c, int i) {
  const unsigned long long v = __hip_atomic_load((const GAS unsigned long long*)(c.w + i), __ATOMIC_RELAXED, __HIP_MEMORY_SCOPE_AGENT);
  return (unsigned)(v >> 32) == c.ep ? (int)(unsigned)v : -1;
}
DEVI void ctl_st(const Ctl& c, int i, int v) {
  __hip_atomic_store((GAS unsigned long long*)(c.w + i), (unsigned long long)c.ep << 32 | (unsigned)v, __ATOMIC_RELAXED, __HIP_MEMORY_SCOPE_AGENT);
}
DEVI void wait_ge(const Ctl& ctl, int p, bool active, int need) {
  for (unsigned sp = 0;; ++sp) {
    const int v = active ? ctl_ld(ctl, p) : need;
    if (__all(v >= need)) break;
    __builtin_amdgcn_s_sleep(4);
    if ((sp & 63u) == 63u) {
      if (__builtin_amdgcn_readfirstlane(ctl_ld(ctl, CTL_TMO)) == 1) break;
      if (sp > SPIN_CAP) { ctl_st(ctl, CTL_TMO, 1); break; }
    }
  }
  __builtin_amdgcn_fence(__ATOMIC_ACQUIRE, "wavefront");
}

DEVI int wait_seg(const Ctl& ctl, int p, bool active, int mul, int add, int need) {
  int have;
  for (unsigned sp = 0;; ++sp) {
    int v = active ? ctl_ld(ctl, p) * mul + add + mul - 1 : 0x7fffffff;
    #pragma unroll
    for (int k = 1; k < 16; k <<= 1) v = min(v, __shfl_xor(v, k));
    have = __builtin_amdgcn_readfirstlane(v);
    if (have >= need) break;
    __builtin_amdgcn_s_sleep(2);
    if ((sp & 63u) == 63u) {
      if (__builtin_amdgcn_readfirstlane(ctl_ld(ctl, CTL_TMO)) == 1) break;
      if (sp > SPIN_CAP) { ctl_st(ctl, CTL_TMO, 1); break; }
    }
  }
  __builtin_amdgcn_fence(__ATOMIC_ACQUIRE, "wavefront");
  return have;
}

DEVI Halves wait_halves(const Ctl& ctl, int lane) {
  int v[6];
  for (unsigned sp = 0;; ++sp) {
    #pragma unroll
    for (int k = 0; k < 3; ++k) { v[k] = ctl_ld(ctl, CTL_PG + (20 + lane) * 8 + 2 * k); v[3 + k] = ctl_ld(ctl, CTL_PG + 2 * 8 + 2 * k); }
    if (__all((v[0] | v[1] | v[2] | v[3] | v[4] | v[5]) >= 0)) break;
    if (sp > SPIN_CAP) { ctl_st(ctl, CTL_TMO, 1); break; }
  }
  auto second_far = [](int a, int b, int c) { return min(min(a & 0xffff, b & 0xffff), c & 0xffff) < (min(min(a, b), c) >> 16); };
  const unsigned long long far = __ballot(second_far(v[0], v[1], v[2]));
  const int c = __ballot(second_far(v[3], v[4], v[5])) ? 2048 : 0;
  return Halves{(unsigned)far & 0xffffu, (unsigned)(far >> 16) & 0xffffu, (unsigned)(far >> 32) & 0xffffu, (unsigned)(far >> 48), c};
}

enum { EPI_AK = 0, EPI_AV = 1, EPI_BQK = 2, EPI_BV = 3, EPI_BOUT = 4 };

template <bool PERM, bool NT = false>
DEVI void load_panel(bf16x8 (&wf)[16][4], const u16* W, int ldw, int col0, LAS char* lds, int w, int lane) {
  const int g = lane >> 4, c = lane & 15, q4 = c >> 2, p4 = c & 3;
  const rsrc_t rw = mk_rs(W, (unsigned)(512 * ldw));
  const unsigned lds0 = (unsigned)(size_t)lds;
  int vo[4];
  #pragma unroll
  for (int n = 0; n < 4; ++n) {
    const int row = 16 * w + 4 * n + g;
    const int f = (row & 3) | ((((PERM ? row >> 2 : row >> 3)) & 1) << 2);
    vo[n] = row * ldw + col0 * 2 + ((c ^ (2 * f)) * 16) - n * 1024;
  }
  int so = 0; unsigned ld = lds0 + w * 4096;
  #pragma unroll
  for (int k = 0; k < 8; ++k) {
    #pragma unroll
    for (int n = 0; n < 4; ++n) dma16n<NT ? 2 : 0>(n, rw, vo[n], so, ld);
    so += 64 * ldw; ld += 16384;
    asm volatile("" : "+s"(so), "+s"(ld));
  }
  const int rb = (PERM ? 16 * (g >> 1) + 4 * (g & 1) : 8 * g) + q4;
  const int f = (rb & 3) | ((g & 1) << 2);
  asm volatile("s_waitcnt vmcnt(0)\n\ts_barrier" ::: "memory");
  #pragma unroll
  for (int t = 0; t < 4; ++t) {
    const LAS char* a = lds + rb * 256 + (((2 * (4 * (w & 1) + t) + (p4 >> 1)) ^ (2 * f)) * 16) + (p4 & 1) * 8;
    const LAS char* b = a + 65536;
    #pragma unroll
    for (int k = 0; k < 8; ++k) {
      wf[k][t] = cat8(trd(a + k * 8192), trd(a + k * 8192 + (PERM ? 8 : 4) * 256));
      wf[8 + k][t] = cat8(trd(b + k * 8192), trd(b + k * 8192 + (PERM ? 8 : 4) * 256));
      if (t < 3) { asm volatile("" : "+a"(wf[k][t])); asm volatile("" : "+a"(wf[8 + k][t])); }
    }
  }
  lds_barrier();
}

struct XTile { rsrc_t rx; int vx[16]; int ao[4]; unsigned lds0; };
DEVI void xtile_init(XTile& t, const void* X, LAS char* lds, int w, int lane) {
  t.rx = mk_rs(X, SEQ * 1024); t.lds0 = (unsigned)(size_t)lds;
  #pragma unroll
  for (int n = 0; n < 16; ++n) t.vx[n] = (lane ^ n) * 16;
  #pragma unroll
  for (int j = 0; j < 4; ++j) t.ao[j] = (w >> 1) * 32768 + (lane & 15) * 1024 + (((4 * j + (lane >> 4)) ^ (lane & 15)) * 16);
}
template <bool SC1>
DEVI void xtile_fetch(const XTile& t, int pair, int buf, int w) {
  int so = (pair * 64 + 16 * w) * 1024; unsigned ld = t.lds0 + buf * 65536 + 16 * w * 1024;
  #pragma unroll
  for (int n = 0; n < 16; ++n) {
    dma16n<SC1>(n & 3, t.rx, t.vx[n], so, ld);
    if ((n & 3) == 3) { so += 4096; ld += 4096; asm volatile("" : "+s"(so), "+s"(ld)); }
  }
}
template <bool XA, bool SC1>
DEVI void xtile_mma(f32x4 (&acc)[2][4], const bf16x8 (&wf)[16][4], const XTile& t, int buf, int nxt, LAS char* lds, int w, const Ctl& ctl, int prog = 0, int done = 0) {
  const LAS char* xb = lds + buf * 65536;
  int so = (nxt * 64 + 16 * w) * 1024;
  unsigned ld = t.lds0 + (buf ^ 1) * 65536 + 16 * w * 1024;
  bf16x8 xf[32];
  auto rd = [&](int n) { xf[n] = *(const LAS bf16x8*)(xb + t.ao[(n >> 1) & 3] + (n & 1) * 16384 + (n >> 3) * 256); };
  __builtin_amdgcn_sched_barrier(0);
  #pragma unroll
  for (int n = 0; n < 4; ++n) rd(n);
  __builtin_amdgcn_sched_barrier(0);
  #pragma unroll
  for (int n = 0; n < 32; ++n) {
    if (n + 4 < 32) rd(n + 4);
    #pragma unroll
    for (int k = 0; k < 4; ++k) acc[n & 1][k] = XA ? MF32(xf[n], wf[n >> 1][k], acc[n & 1][k]) : MF32(wf[n >> 1][k], xf[n], acc[n & 1][k]);
    __builtin_amdgcn_sched_group_barrier(0x8, 1, 0); __builtin_amdgcn_sched_group_barrier(0x100, 1, 0); __builtin_amdgcn_sched_group_barrier(0x8, 3, 0);
    __builtin_amdgcn_sched_barrier(0);
    if (n < 24 && n % 3 != 2) {
      dma16n<SC1>((n - n / 3) & 3, t.rx, t.vx[n - n / 3], so, ld);
      if (((n - n / 3) & 3) == 3) { so += 4096; ld += 4096; asm volatile("" : "+s"(so), "+s"(ld)); }
      __builtin_amdgcn_sched_barrier(0);
    }
    if (n == 11 && prog) {
      asm volatile("s_waitcnt vmcnt(8)" ::: "memory");
      ctl_st(ctl, prog, done);
      __builtin_amdgcn_sched_barrier(0);
    }
  }
}

template <int EPI>
DEVI void gemm_pairs(const bf16x8 (&wf)[16][4], const void* X, int pair0, int pstep, int npair, rsrc_t rout, int so0, float scale, const Ctl& ctl, int prog, LAS char* lds, int w, int lane) {
  constexpr bool SC1 = EPI == EPI_BOUT;
  constexpr int NST = (EPI == EPI_AK || EPI == EPI_AV) ? 4 : 8;
  const int g = lane >> 4, c = lane & 15, wr = w >> 1, wc = w & 1;
  XTile xt; xtile_init(xt, X, lds, w, lane);
  xtile_fetch<SC1>(xt, pair0, 0, w);
  if (EPI == EPI_AK || EPI == EPI_AV) { const Halves hv = wait_halves(ctl, lane); so0 |= (EPI == EPI_AK ? hv.k : hv.v) << 8; }
  for (int i = 0; i < npair; ++i) {
    const int pair = pair0 + i * pstep, seg = 2 * pair + wr;
    if (i == 0) asm volatile("s_waitcnt vmcnt(0)\n\ts_barrier" ::: "memory");
    else asm volatile("s_waitcnt vmcnt(%0)\n\ts_barrier" :: "i"(NST) : "memory");
    f32x4 acc[2][4] = {};
    xtile_mma<EPI == EPI_AV || EPI == EPI_BV, SC1>(acc, wf, xt, i & 1, i + 1 < npair ? pair + pstep : pair, lds, w, ctl, prog, i > 0 ? i - 1 : -1);
    u32x2 pk[2][4];
    #pragma unroll
    for (int T = 0; T < 2; ++T)
      #pragma unroll
      for (int t = 0; t < 4; ++t) {
        if constexpr (EPI == EPI_BQK) acc[T][t] *= scale;
        pk[T][t] = (u32x2){pk2(acc[T][t][0], acc[T][t][1]), pk2(acc[T][t][2], acc[T][t][3])};
      }
    if constexpr (EPI == EPI_AK) {
      #pragma unroll
      for (int T = 0; T < 2; ++T)
        #pragma unroll
        for (int k = 0; k < 2; ++k) st16((u32x4){pk[T][2 * k].x, pk[T][2 * k].y, pk[T][2 * k + 1].x, pk[T][2 * k + 1].y}, rout, lane * 16 + k * 1024, far_off(seg, so0 & 31, (unsigned)so0 >> 8) + (T * 4 + 2 * wc) * 1024);
    } else if constexpr (EPI == EPI_AV) {
      #pragma unroll
      for (int t = 0; t < 4; ++t) st16((u32x4){pk[0][t].x, pk[0][t].y, pk[1][t].x, pk[1][t].y}, rout, lane * 16 + t * 1024, far_off(seg, so0 & 31, (unsigned)so0 >> 8) + 4 * wc * 1024);
    } else if constexpr (EPI == EPI_BQK) {
      #pragma unroll
      for (int T = 0; T < 2; ++T)
        #pragma unroll
        for (int t = 0; t < 4; ++t) st8(pk[T][t], rout, c * 32 + g * 8 + T * 512, so0 + (4 * wc + t) * 65536 + seg * 1024);
    } else if constexpr (EPI == EPI_BV) {
      #pragma unroll
      for (int T = 0; T < 2; ++T)
        #pragma unroll
        for (int t = 0; t < 4; ++t) st8(pk[T][t], rout, ((g & 1) * 16 + c) * 16 + (g >> 1) * 8 + T * 512, so0 + (4 * wc + t) * 65536 + seg * 1024);
    } else {
      #pragma unroll
      for (int T = 0; T < 2; ++T)
        #pragma unroll
        for (int t = 0; t < 4; ++t) __builtin_amdgcn_raw_buffer_store_b64(pk[T][t], rout, c * 2048 + g * 8 + t * 32, so0 + (seg * 32 + 16 * T) * 2048 + 128 * wc, 0);
    }
    if (i == 0 && (EPI == EPI_AK || EPI == EPI_AV)) {
      asm volatile("s_waitcnt vmcnt(0)" ::: "memory");
      ctl_st(ctl, prog, 0);
    }
  }
  asm volatile("s_waitcnt vmcnt(0)\n\ts_barrier" ::: "memory");
  if (prog) ctl_st(ctl, prog, npair - 1);
}

DEVI void attn_a_wg(const Params& p, const Ctl& ctl, const bf16x8 (&wf)[16][4], int h, const Halves hv, LAS char* lds, int w, int lane) {
  const int g = lane >> 4, c = lane & 15, wr = w >> 1, Tq = w & 1;
  const rsrc_t rx = mk_rs(p.ws + W_KX, 64u << 20), rm = mk_rs(p.ws + W_KM, 32u << 20), ro = mk_rs(p.ws + W_O, 32u << 20);
  const float c1 = 0.08838834764831845f * 1.4426950408889634f;
  const u32x4 ones = {0x3f803f80u, 0x3f803f80u, 0x3f803f80u, 0x3f803f80u};
  const f32x4 z4 = {0.f, 0.f, 0.f, 0.f};
  int have_x = -1, have_m = -1;
  XTile xt; xtile_init(xt, p.ax, lds, w, lane);
  xtile_fetch<false>(xt, 0, 0, w);
  LAS char* ex = lds + 131072 + wr * 8192 + lane * 16;
  for (int i = 0; i < 32; ++i) {
    const int s = 2 * i + wr;
    asm volatile("s_waitcnt vmcnt(0)\n\ts_barrier" ::: "memory");
    f32x4 acc[2][4] = {};
    xtile_mma<false, false>(acc, wf, xt, i & 1, i < 31 ? i + 1 : i, lds, w, ctl);
    #pragma unroll
    for (int T = 0; T < 2; ++T)
      #pragma unroll
      for (int k = 0; k < 2; ++k)
        *(LAS u32x4*)(ex + (T * 4 + 2 * Tq + k) * 1024) = (u32x4){pk2(acc[T][2 * k][0], acc[T][2 * k][1]), pk2(acc[T][2 * k][2], acc[T][2 * k][3]),
                                                                pk2(acc[T][2 * k + 1][0], acc[T][2 * k + 1][1]), pk2(acc[T][2 * k + 1][2], acc[T][2 * k + 1][3])};
    if (have_x < s) have_x = wait_seg(ctl, CTL_E1 + ((lane >> 1) * 32 + h) * 16 + (2 * wr + (lane & 1)) * 4, lane < 4, 2, 0, s);
    const int so = far_off(s, h, hv.k), sv = far_off(s, h, hv.v), sm_ = far_off(s, h, hv.m);
    u32x4 kf[8], vf[8];
    #pragma unroll
    for (int k = 0; k < 8; ++k) kf[k] = ld16(rx, lane * 16 + (k & 3) * 1024, so + (k >> 2) * 4096);
    #pragma unroll
    for (int k = 0; k < 8; ++k) vf[k] = ld16(rx, lane * 16 + (k & 3) * 1024, (32 << 20) + sv + (k >> 2) * 4096);
    lds_barrier();
    bf16x8 qf[4];
    #pragma unroll
    for (int k = 0; k < 4; ++k) qf[k] = *(LAS bf16x8*)(ex + (Tq * 4 + k) * 1024);
    f32x4 s0 = z4, s1 = z4;
    #pragma unroll
    for (int k = 0; k < 4; ++k) { s0 = MF32(as_bf8(kf[k]), qf[k], s0); s1 = MF32(as_bf8(kf[4 + k]), qf[k], s1); }
    #pragma unroll
    for (int r = 0; r < 4; ++r) {
      if (4 * g + r > 16 * Tq + c) s0[r] = -INFINITY;
      if (16 + 4 * g + r > 16 * Tq + c) s1[r] = -INFINITY;
    }
    float mx = fmaxf(fmaxf(fmaxf(s0[0], s0[1]), fmaxf(s0[2], s0[3])), fmaxf(fmaxf(s1[0], s1[1]), fmaxf(s1[2], s1[3])));
    __builtin_amdgcn_sched_barrier(0);
    if (have_m < s) have_m = wait_seg(ctl, CTL_SEG + hv.c + (h + 32 * lane) * 32, lane < 2, 1, 0, s);
    u32x4 km[4], vm[4];
    #pragma unroll
    for (int k = 0; k < 4; ++k) km[k] = ld16(rm, lane * 16 + k * 1024, sm_);
    #pragma unroll
    for (int k = 0; k < 4; ++k) vm[k] = ld16(rm, lane * 16 + k * 1024, sm_ + 4096);
    __builtin_amdgcn_sched_barrier(0);
    f32x4 sm = z4;
    #pragma unroll
    for (int k = 0; k < 4; ++k) sm = MF32(as_bf8(km[k]), qf[k], sm);
    mx = fmaxf(mx, fmaxf(sm[0], sm[1]));
    float o; mx = swap16(mx, o); mx = fmaxf(mx, o); mx = swap32(mx, o); mx = fmaxf(mx, o);
    const float nm = -mx * c1;
    auto ex2 = [&](float x) { return __builtin_amdgcn_exp2f(fmaf(x, c1, nm)); };
    const u32x4 px = {pk2(ex2(s0[0]), ex2(s0[1])), pk2(ex2(s0[2]), ex2(s0[3])), pk2(ex2(s1[0]), ex2(s1[1])), pk2(ex2(s1[2]), ex2(s1[3]))};
    const u32x4 pm = {pk2(ex2(sm[0]), ex2(sm[1])), 0u, 0u, 0u};
    f32x4 l = MF32(as_bf8(ones), as_bf8(px), z4);
    l = MF32(as_bf8(ones), as_bf8(pm), l);
    const float inv = __builtin_amdgcn_rcpf(l[0]);
    const int oo = far_off(s, 16 * Tq + (h >> 1), hv.o) + (h & 1) * 4096;
    #pragma unroll
    for (int tp = 0; tp < 4; ++tp) {
      f32x4 o0 = MF32(as_bf8(vf[2 * tp]), as_bf8(px), z4), o1 = MF32(as_bf8(vf[2 * tp + 1]), as_bf8(px), z4);
      o0 = MF32(as_bf8(((u32x4){vm[tp].x, 0u, 0u, 0u})), as_bf8(pm), o0);
      o1 = MF32(as_bf8(((u32x4){vm[tp].z, 0u, 0u, 0u})), as_bf8(pm), o1);
      st16((u32x4){pk2(o0[0] * inv, o0[1] * inv), pk2(o0[2] * inv, o0[3] * inv), pk2(o1[0] * inv, o1[1] * inv), pk2(o1[2] * inv, o1[3] * inv)}, ro, lane * 16 + tp * 1024, oo);
    }
    asm volatile("s_waitcnt vmcnt(0)" ::: "memory");
    if (lane == 0) ctl_st(ctl, CTL_ATA + hv.c + ((Tq * 32 + h) * 2 + wr) * 8, i);
  }
}

#define OPROJ_ASM \
  "s_lshl_b32 s54, s51, 20\n\t" \
  "s_lshl_b32 s58, s51, 15\n\t" \
  "s_add_u32 m0, s58, 0\n\t" \
  "s_add_u32 s55, s54, 0\n\t" \
  "buffer_load_dwordx4 v24, s[60:63], s55 offen lds\n\t" \
  "s_add_u32 m0, s58, 1024\n\t" \
  "s_add_u32 s55, s54, 16384\n\t" \
  "buffer_load_dwordx4 v24, s[60:63], s55 offen lds\n\t" \
  "s_add_u32 m0, s58, 2048\n\t" \
  "s_add_u32 s55, s54, 32768\n\t" \
  "buffer_load_dwordx4 v24, s[60:63], s55 offen lds\n\t" \
  "s_add_u32 m0, s58, 3072\n\t" \
  "s_add_u32 s55, s54, 49152\n\t" \
  "buffer_load_dwordx4 v24, s[60:63], s55 offen lds\n\t" \
  "s_add_u32 m0, s58, 4096\n\t" \
  "s_add_u32 s55, s54, 65536\n\t" \
  "buffer_load_dwordx4 v24, s[60:63], s55 offen lds\n\t" \
  "s_add_u32 m0, s58, 5120\n\t" \
  "s_add_u32 s55, s54, 81920\n\t" \
  "buffer_load_dwordx4 v24, s[60:63], s55 offen lds\n\t" \
  "s_add_u32 m0, s58, 6144\n\t" \
  "s_add_u32 s55, s54, 98304\n\t" \
  "buffer_load_dwordx4 v24, s[60:63], s55 offen lds\n\t" \
  "s_add_u32 m0, s58, 7168\n\t" \
  "s_add_u32 s55, s54, 114688\n\t" \
  "buffer_load_dwordx4 v24, s[60:63], s55 offen lds\n\t" \
  "s_add_u32 m0, s58, 8192\n\t" \
  "s_add_u32 s55, s54, 131072\n\t" \
  "buffer_load_dwordx4 v24, s[60:63], s55 offen lds\n\t" \
  "s_add_u32 m0, s58, 9216\n\t" \
  "s_add_u32 s55, s54, 147456\n\t" \
  "buffer_load_dwordx4 v24, s[60:63], s55 offen lds\n\t" \
  "s_add_u32 m0, s58, 10240\n\t" \
  "s_add_u32 s55, s54, 163840\n\t" \
  "buffer_load_dwordx4 v24, s[60:63], s55 offen lds\n\t" \
  "s_add_u32 m0, s58, 11264\n\t" \
  "s_add_u32 s55, s54, 180224\n\t" \
  "buffer_load_dwordx4 v24, s[60:63], s55 offen lds\n\t" \
  "s_add_u32 m0, s58, 12288\n\t" \
  "s_add_u32 s55, s54, 196608\n\t" \
  "buffer_load_dwordx4 v24, s[60:63], s55 offen lds\n\t" \
  "s_add_u32 m0, s58, 13312\n\t" \
  "s_add_u32 s55, s54, 212992\n\t" \
  "buffer_load_dwordx4 v24, s[60:63], s55 offen lds\n\t" \
  "s_add_u32 m0, s58, 14336\n\t" \
  "s_add_u32 s55, s54, 229376\n\t" \
  "buffer_load_dwordx4 v24, s[60:63], s55 offen lds\n\t" \
  "s_add_u32 m0, s58, 15360\n\t" \
  "s_add_u32 s55, s54, 245760\n\t" \
  "buffer_load_dwordx4 v24, s[60:63], s55 offen lds\n\t" \
  "s_add_u32 s54, s54, 262144\n\t" \
  "s_add_u32 m0, s58, 16384\n\t" \
  "s_add_u32 s55, s54, 0\n\t" \
  "buffer_load_dwordx4 v24, s[60:63], s55 offen lds\n\t" \
  "s_add_u32 m0, s58, 17408\n\t" \
  "s_add_u32 s55, s54, 16384\n\t" \
  "buffer_load_dwordx4 v24, s[60:63], s55 offen lds\n\t" \
  "s_add_u32 m0, s58, 18432\n\t" \
  "s_add_u32 s55, s54, 32768\n\t" \
  "buffer_load_dwordx4 v24, s[60:63], s55 offen lds\n\t" \
  "s_add_u32 m0, s58, 19456\n\t" \
  "s_add_u32 s55, s54, 49152\n\t" \
  "buffer_load_dwordx4 v24, s[60:63], s55 offen lds\n\t" \
  "s_add_u32 m0, s58, 20480\n\t" \
  "s_add_u32 s55, s54, 65536\n\t" \
  "buffer_load_dwordx4 v24, s[60:63], s55 offen lds\n\t" \
  "s_add_u32 m0, s58, 21504\n\t" \
  "s_add_u32 s55, s54, 81920\n\t" \
  "buffer_load_dwordx4 v24, s[60:63], s55 offen lds\n\t" \
  "s_add_u32 m0, s58, 22528\n\t" \
  "s_add_u32 s55, s54, 98304\n\t" \
  "buffer_load_dwordx4 v24, s[60:63], s55 offen lds\n\t" \
  "s_add_u32 m0, s58, 23552\n\t" \
  "s_add_u32 s55, s54, 114688\n\t" \
  "buffer_load_dwordx4 v24, s[60:63], s55 offen lds\n\t" \
  "s_add_u32 m0, s58, 24576\n\t" \
  "s_add_u32 s55, s54, 131072\n\t" \
  "buffer_load_dwordx4 v24, s[60:63], s55 offen lds\n\t" \
  "s_add_u32 m0, s58, 25600\n\t" \
  "s_add_u32 s55, s54, 147456\n\t" \
  "buffer_load_dwordx4 v24, s[60:63], s55 offen lds\n\t" \
  "s_add_u32 m0, s58, 26624\n\t" \
  "s_add_u32 s55, s54, 163840\n\t" \
  "buffer_load_dwordx4 v24, s[60:63], s55 offen lds\n\t" \
  "s_add_u32 m0, s58, 27648\n\t" \
  "s_add_u32 s55, s54, 180224\n\t" \
  "buffer_load_dwordx4 v24, s[60:63], s55 offen lds\n\t" \
  "s_add_u32 m0, s58, 28672\n\t" \
  "s_add_u32 s55, s54, 196608\n\t" \
  "buffer_load_dwordx4 v24, s[60:63], s55 offen lds\n\t" \
  "s_add_u32 m0, s58, 29696\n\t" \
  "s_add_u32 s55, s54, 212992\n\t" \
  "buffer_load_dwordx4 v24, s[60:63], s55 offen lds\n\t" \
  "s_add_u32 m0, s58, 30720\n\t" \
  "s_add_u32 s55, s54, 229376\n\t" \
  "buffer_load_dwordx4 v24, s[60:63], s55 offen lds\n\t" \
  "s_add_u32 m0, s58, 31744\n\t" \
  "s_add_u32 s55, s54, 245760\n\t" \
  "buffer_load_dwordx4 v24, s[60:63], s55 offen lds\n\t" \
  "s_add_u32 s54, s54, 262144\n\t" \
  "s_waitcnt vmcnt(16)\n\t" \
  "ds_read_b64_tr_b16 a[0:1], v22 offset:0\n\t" \
  "ds_read_b64_tr_b16 a[2:3], v22 offset:1024\n\t" \
  "ds_read_b64_tr_b16 a[4:5], v23 offset:0\n\t" \
  "ds_read_b64_tr_b16 a[6:7], v23 offset:1024\n\t" \
  "ds_read_b64_tr_b16 a[8:9], v22 offset:2048\n\t" \
  "ds_read_b64_tr_b16 a[10:11], v22 offset:3072\n\t" \
  "ds_read_b64_tr_b16 a[12:13], v23 offset:2048\n\t" \
  "ds_read_b64_tr_b16 a[14:15], v23 offset:3072\n\t" \
  "ds_read_b64_tr_b16 a[16:17], v22 offset:4096\n\t" \
  "ds_read_b64_tr_b16 a[18:19], v22 offset:5120\n\t" \
  "ds_read_b64_tr_b16 a[20:21], v23 offset:4096\n\t" \
  "ds_read_b64_tr_b16 a[22:23], v23 offset:5120\n\t" \
  "ds_read_b64_tr_b16 a[24:25], v22 offset:6144\n\t" \
  "ds_read_b64_tr_b16 a[26:27], v22 offset:7168\n\t" \
  "ds_read_b64_tr_b16 a[28:29], v23 offset:6144\n\t" \
  "ds_read_b64_tr_b16 a[30:31], v23 offset:7168\n\t" \
  "ds_read_b64_tr_b16 a[32:33], v22 offset:8192\n\t" \
  "ds_read_b64_tr_b16 a[34:35], v22 offset:9216\n\t" \
  "ds_read_b64_tr_b16 a[36:37], v23 offset:8192\n\t" \
  "ds_read_b64_tr_b16 a[38:39], v23 offset:9216\n\t" \
  "ds_read_b64_tr_b16 a[40:41], v22 offset:10240\n\t" \
  "ds_read_b64_tr_b16 a[42:43], v22 offset:11264\n\t" \
  "ds_read_b64_tr_b16 a[44:45], v23 offset:10240\n\t" \
  "ds_read_b64_tr_b16 a[46:47], v23 offset:11264\n\t" \
  "ds_read_b64_tr_b16 a[48:49], v22 offset:12288\n\t" \
  "ds_read_b64_tr_b16 a[50:51], v22 offset:13312\n\t" \
  "ds_read_b64_tr_b16 a[52:53], v23 offset:12288\n\t" \
  "ds_read_b64_tr_b16 a[54:55], v23 offset:13312\n\t" \
  "ds_read_b64_tr_b16 a[56:57], v22 offset:14336\n\t" \
  "ds_read_b64_tr_b16 a[58:59], v22 offset:15360\n\t" \
  "ds_read_b64_tr_b16 a[60:61], v23 offset:14336\n\t" \
  "ds_read_b64_tr_b16 a[62:63], v23 offset:15360\n\t" \
  "s_waitcnt lgkmcnt(0)\n\t" \
  "s_add_u32 m0, s58, 0\n\t" \
  "s_add_u32 s55, s54, 0\n\t" \
  "buffer_load_dwordx4 v24, s[60:63], s55 offen lds\n\t" \
  "s_add_u32 m0, s58, 1024\n\t" \
  "s_add_u32 s55, s54, 16384\n\t" \
  "buffer_load_dwordx4 v24, s[60:63], s55 offen lds\n\t" \
  "s_add_u32 m0, s58, 2048\n\t" \
  "s_add_u32 s55, s54, 32768\n\t" \
  "buffer_load_dwordx4 v24, s[60:63], s55 offen lds\n\t" \
  "s_add_u32 m0, s58, 3072\n\t" \
  "s_add_u32 s55, s54, 49152\n\t" \
  "buffer_load_dwordx4 v24, s[60:63], s55 offen lds\n\t" \
  "s_add_u32 m0, s58, 4096\n\t" \
  "s_add_u32 s55, s54, 65536\n\t" \
  "buffer_load_dwordx4 v24, s[60:63], s55 offen lds\n\t" \
  "s_add_u32 m0, s58, 5120\n\t" \
  "s_add_u32 s55, s54, 81920\n\t" \
  "buffer_load_dwordx4 v24, s[60:63], s55 offen lds\n\t" \
  "s_add_u32 m0, s58, 6144\n\t" \
  "s_add_u32 s55, s54, 98304\n\t" \
  "buffer_load_dwordx4 v24, s[60:63], s55 offen lds\n\t" \
  "s_add_u32 m0, s58, 7168\n\t" \
  "s_add_u32 s55, s54, 114688\n\t" \
  "buffer_load_dwordx4 v24, s[60:63], s55 offen lds\n\t" \
  "s_add_u32 m0, s58, 8192\n\t" \
  "s_add_u32 s55, s54, 131072\n\t" \
  "buffer_load_dwordx4 v24, s[60:63], s55 offen lds\n\t" \
  "s_add_u32 m0, s58, 9216\n\t" \
  "s_add_u32 s55, s54, 147456\n\t" \
  "buffer_load_dwordx4 v24, s[60:63], s55 offen lds\n\t" \
  "s_add_u32 m0, s58, 10240\n\t" \
  "s_add_u32 s55, s54, 163840\n\t" \
  "buffer_load_dwordx4 v24, s[60:63], s55 offen lds\n\t" \
  "s_add_u32 m0, s58, 11264\n\t" \
  "s_add_u32 s55, s54, 180224\n\t" \
  "buffer_load_dwordx4 v24, s[60:63], s55 offen lds\n\t" \
  "s_add_u32 m0, s58, 12288\n\t" \
  "s_add_u32 s55, s54, 196608\n\t" \
  "buffer_load_dwordx4 v24, s[60:63], s55 offen lds\n\t" \
  "s_add_u32 m0, s58, 13312\n\t" \
  "s_add_u32 s55, s54, 212992\n\t" \
  "buffer_load_dwordx4 v24, s[60:63], s55 offen lds\n\t" \
  "s_add_u32 m0, s58, 14336\n\t" \
  "s_add_u32 s55, s54, 229376\n\t" \
  "buffer_load_dwordx4 v24, s[60:63], s55 offen lds\n\t" \
  "s_add_u32 m0, s58, 15360\n\t" \
  "s_add_u32 s55, s54, 245760\n\t" \
  "buffer_load_dwordx4 v24, s[60:63], s55 offen lds\n\t" \
  "s_add_u32 s54, s54, 262144\n\t" \
  "s_waitcnt vmcnt(16)\n\t" \
  "ds_read_b64_tr_b16 a[64:65], v22 offset:16384\n\t" \
  "ds_read_b64_tr_b16 a[66:67], v22 offset:17408\n\t" \
  "ds_read_b64_tr_b16 a[68:69], v23 offset:16384\n\t" \
  "ds_read_b64_tr_b16 a[70:71], v23 offset:17408\n\t" \
  "ds_read_b64_tr_b16 a[72:73], v22 offset:18432\n\t" \
  "ds_read_b64_tr_b16 a[74:75], v22 offset:19456\n\t" \
  "ds_read_b64_tr_b16 a[76:77], v23 offset:18432\n\t" \
  "ds_read_b64_tr_b16 a[78:79], v23 offset:19456\n\t" \
  "ds_read_b64_tr_b16 a[80:81], v22 offset:20480\n\t" \
  "ds_read_b64_tr_b16 a[82:83], v22 offset:21504\n\t" \
  "ds_read_b64_tr_b16 a[84:85], v23 offset:20480\n\t" \
  "ds_read_b64_tr_b16 a[86:87], v23 offset:21504\n\t" \
  "ds_read_b64_tr_b16 a[88:89], v22 offset:22528\n\t" \
  "ds_read_b64_tr_b16 a[90:91], v22 offset:23552\n\t" \
  "ds_read_b64_tr_b16 a[92:93], v23 offset:22528\n\t" \
  "ds_read_b64_tr_b16 a[94:95], v23 offset:23552\n\t" \
  "ds_read_b64_tr_b16 a[96:97], v22 offset:24576\n\t" \
  "ds_read_b64_tr_b16 a[98:99], v22 offset:25600\n\t" \
  "ds_read_b64_tr_b16 a[100:101], v23 offset:24576\n\t" \
  "ds_read_b64_tr_b16 a[102:103], v23 offset:25600\n\t" \
  "ds_read_b64_tr_b16 a[104:105], v22 offset:26624\n\t" \
  "ds_read_b64_tr_b16 a[106:107], v22 offset:27648\n\t" \
  "ds_read_b64_tr_b16 a[108:109], v23 offset:26624\n\t" \
  "ds_read_b64_tr_b16 a[110:111], v23 offset:27648\n\t" \
  "ds_read_b64_tr_b16 a[112:113], v22 offset:28672\n\t" \
  "ds_read_b64_tr_b16 a[114:115], v22 offset:29696\n\t" \
  "ds_read_b64_tr_b16 a[116:117], v23 offset:28672\n\t" \
  "ds_read_b64_tr_b16 a[118:119], v23 offset:29696\n\t" \
  "ds_read_b64_tr_b16 a[120:121], v22 offset:30720\n\t" \
  "ds_read_b64_tr_b16 a[122:123], v22 offset:31744\n\t" \
  "ds_read_b64_tr_b16 a[124:125], v23 offset:30720\n\t" \
  "ds_read_b64_tr_b16 a[126:127], v23 offset:31744\n\t" \
  "s_waitcnt lgkmcnt(0)\n\t" \
  "s_add_u32 m0, s58, 16384\n\t" \
  "s_add_u32 s55, s54, 0\n\t" \
  "buffer_load_dwordx4 v24, s[60:63], s55 offen lds\n\t" \
  "s_add_u32 m0, s58, 17408\n\t" \
  "s_add_u32 s55, s54, 16384\n\t" \
  "buffer_load_dwordx4 v24, s[60:63], s55 offen lds\n\t" \
  "s_add_u32 m0, s58, 18432\n\t" \
  "s_add_u32 s55, s54, 32768\n\t" \
  "buffer_load_dwordx4 v24, s[60:63], s55 offen lds\n\t" \
  "s_add_u32 m0, s58, 19456\n\t" \
  "s_add_u32 s55, s54, 49152\n\t" \
  "buffer_load_dwordx4 v24, s[60:63], s55 offen lds\n\t" \
  "s_add_u32 m0, s58, 20480\n\t" \
  "s_add_u32 s55, s54, 65536\n\t" \
  "buffer_load_dwordx4 v24, s[60:63], s55 offen lds\n\t" \
  "s_add_u32 m0, s58, 21504\n\t" \
  "s_add_u32 s55, s54, 81920\n\t" \
  "buffer_load_dwordx4 v24, s[60:63], s55 offen lds\n\t" \
  "s_add_u32 m0, s58, 22528\n\t" \
  "s_add_u32 s55, s54, 98304\n\t" \
  "buffer_load_dwordx4 v24, s[60:63], s55 offen lds\n\t" \
  "s_add_u32 m0, s58, 23552\n\t" \
  "s_add_u32 s55, s54, 114688\n\t" \
  "buffer_load_dwordx4 v24, s[60:63], s55 offen lds\n\t" \
  "s_add_u32 m0, s58, 24576\n\t" \
  "s_add_u32 s55, s54, 131072\n\t" \
  "buffer_load_dwordx4 v24, s[60:63], s55 offen lds\n\t" \
  "s_add_u32 m0, s58, 25600\n\t" \
  "s_add_u32 s55, s54, 147456\n\t" \
  "buffer_load_dwordx4 v24, s[60:63], s55 offen lds\n\t" \
  "s_add_u32 m0, s58, 26624\n\t" \
  "s_add_u32 s55, s54, 163840\n\t" \
  "buffer_load_dwordx4 v24, s[60:63], s55 offen lds\n\t" \
  "s_add_u32 m0, s58, 27648\n\t" \
  "s_add_u32 s55, s54, 180224\n\t" \
  "buffer_load_dwordx4 v24, s[60:63], s55 offen lds\n\t" \
  "s_add_u32 m0, s58, 28672\n\t" \
  "s_add_u32 s55, s54, 196608\n\t" \
  "buffer_load_dwordx4 v24, s[60:63], s55 offen lds\n\t" \
  "s_add_u32 m0, s58, 29696\n\t" \
  "s_add_u32 s55, s54, 212992\n\t" \
  "buffer_load_dwordx4 v24, s[60:63], s55 offen lds\n\t" \
  "s_add_u32 m0, s58, 30720\n\t" \
  "s_add_u32 s55, s54, 229376\n\t" \
  "buffer_load_dwordx4 v24, s[60:63], s55 offen lds\n\t" \
  "s_add_u32 m0, s58, 31744\n\t" \
  "s_add_u32 s55, s54, 245760\n\t" \
  "buffer_load_dwordx4 v24, s[60:63], s55 offen lds\n\t" \
  "s_add_u32 s54, s54, 262144\n\t" \
  "s_waitcnt vmcnt(16)\n\t" \
  "ds_read_b64_tr_b16 a[128:129], v22 offset:0\n\t" \
  "ds_read_b64_tr_b16 a[130:131], v22 offset:1024\n\t" \
  "ds_read_b64_tr_b16 a[132:133], v23 offset:0\n\t" \
  "ds_read_b64_tr_b16 a[134:135], v23 offset:1024\n\t" \
  "ds_read_b64_tr_b16 a[136:137], v22 offset:2048\n\t" \
  "ds_read_b64_tr_b16 a[138:139], v22 offset:3072\n\t" \
  "ds_read_b64_tr_b16 a[140:141], v23 offset:2048\n\t" \
  "ds_read_b64_tr_b16 a[142:143], v23 offset:3072\n\t" \
  "ds_read_b64_tr_b16 a[144:145], v22 offset:4096\n\t" \
  "ds_read_b64_tr_b16 a[146:147], v22 offset:5120\n\t" \
  "ds_read_b64_tr_b16 a[148:149], v23 offset:4096\n\t" \
  "ds_read_b64_tr_b16 a[150:151], v23 offset:5120\n\t" \
  "ds_read_b64_tr_b16 a[152:153], v22 offset:6144\n\t" \
  "ds_read_b64_tr_b16 a[154:155], v22 offset:7168\n\t" \
  "ds_read_b64_tr_b16 a[156:157], v23 offset:6144\n\t" \
  "ds_read_b64_tr_b16 a[158:159], v23 offset:7168\n\t" \
  "ds_read_b64_tr_b16 a[160:161], v22 offset:8192\n\t" \
  "ds_read_b64_tr_b16 a[162:163], v22 offset:9216\n\t" \
  "ds_read_b64_tr_b16 a[164:165], v23 offset:8192\n\t" \
  "ds_read_b64_tr_b16 a[166:167], v23 offset:9216\n\t" \
  "ds_read_b64_tr_b16 a[168:169], v22 offset:10240\n\t" \
  "ds_read_b64_tr_b16 a[170:171], v22 offset:11264\n\t" \
  "ds_read_b64_tr_b16 a[172:173], v23 offset:10240\n\t" \
  "ds_read_b64_tr_b16 a[174:175], v23 offset:11264\n\t" \
  "ds_read_b64_tr_b16 a[176:177], v22 offset:12288\n\t" \
  "ds_read_b64_tr_b16 a[178:179], v22 offset:13312\n\t" \
  "ds_read_b64_tr_b16 a[180:181], v23 offset:12288\n\t" \
  "ds_read_b64_tr_b16 a[182:183], v23 offset:13312\n\t" \
  "ds_read_b64_tr_b16 a[184:185], v22 offset:14336\n\t" \
  "ds_read_b64_tr_b16 a[186:187], v22 offset:15360\n\t" \
  "ds_read_b64_tr_b16 a[188:189], v23 offset:14336\n\t" \
  "ds_read_b64_tr_b16 a[190:191], v23 offset:15360\n\t" \
  "s_waitcnt lgkmcnt(0)\n\t" \
  "s_waitcnt vmcnt(0)\n\t" \
  "ds_read_b64_tr_b16 a[192:193], v22 offset:16384\n\t" \
  "ds_read_b64_tr_b16 a[194:195], v22 offset:17408\n\t" \
  "ds_read_b64_tr_b16 a[196:197], v23 offset:16384\n\t" \
  "ds_read_b64_tr_b16 a[198:199], v23 offset:17408\n\t" \
  "ds_read_b64_tr_b16 a[200:201], v22 offset:18432\n\t" \
  "ds_read_b64_tr_b16 a[202:203], v22 offset:19456\n\t" \
  "ds_read_b64_tr_b16 a[204:205], v23 offset:18432\n\t" \
  "ds_read_b64_tr_b16 a[206:207], v23 offset:19456\n\t" \
  "ds_read_b64_tr_b16 a[208:209], v22 offset:20480\n\t" \
  "ds_read_b64_tr_b16 a[210:211], v22 offset:21504\n\t" \
  "ds_read_b64_tr_b16 a[212:213], v23 offset:20480\n\t" \
  "ds_read_b64_tr_b16 a[214:215], v23 offset:21504\n\t" \
  "ds_read_b64_tr_b16 a[216:217], v22 offset:22528\n\t" \
  "ds_read_b64_tr_b16 a[218:219], v22 offset:23552\n\t" \
  "ds_read_b64_tr_b16 a[220:221], v23 offset:22528\n\t" \
  "ds_read_b64_tr_b16 a[222:223], v23 offset:23552\n\t" \
  "ds_read_b64_tr_b16 a[224:225], v22 offset:24576\n\t" \
  "ds_read_b64_tr_b16 a[226:227], v22 offset:25600\n\t" \
  "ds_read_b64_tr_b16 a[228:229], v23 offset:24576\n\t" \
  "ds_read_b64_tr_b16 a[230:231], v23 offset:25600\n\t" \
  "ds_read_b64_tr_b16 a[232:233], v22 offset:26624\n\t" \
  "ds_read_b64_tr_b16 a[234:235], v22 offset:27648\n\t" \
  "ds_read_b64_tr_b16 a[236:237], v23 offset:26624\n\t" \
  "ds_read_b64_tr_b16 a[238:239], v23 offset:27648\n\t" \
  "ds_read_b64_tr_b16 a[240:241], v22 offset:28672\n\t" \
  "ds_read_b64_tr_b16 a[242:243], v22 offset:29696\n\t" \
  "ds_read_b64_tr_b16 a[244:245], v23 offset:28672\n\t" \
  "ds_read_b64_tr_b16 a[246:247], v23 offset:29696\n\t" \
  "ds_read_b64_tr_b16 a[248:249], v22 offset:30720\n\t" \
  "ds_read_b64_tr_b16 a[250:251], v22 offset:31744\n\t" \
  "ds_read_b64_tr_b16 a[252:253], v23 offset:30720\n\t" \
  "ds_read_b64_tr_b16 a[254:255], v23 offset:31744\n\t" \
  "s_waitcnt lgkmcnt(0)\n\t" \
  "s_barrier\n\t" \
  "s_mov_b32 s52, 0\n\t" \
  "s_mov_b32 s53, -1\n\t" \
  "s_mov_b32 s59, 0\n\t" \
  "Lw_%=:\n\t" \
  "s_mov_b32 s57, 0\n\t" \
  "s_cmp_ge_i32 s53, s52\n\t" \
  "s_cbranch_scc1 Lg_%=\n\t" \
  "Lp_%=:\n\t" \
  "buffer_load_dwordx2 v[168:169], v17, s[44:47], 0 offen sc1\n\t" \
  "s_waitcnt vmcnt(0)\n\t" \
  "v_cmp_eq_u32 vcc, s64, v169\n\t" \
  "s_nop 1\n\t" \
  "v_cndmask_b32 v168, -1, v168, vcc\n\t" \
  "v_lshl_add_u32 v168, v168, 1, v18\n\t" \
  "s_nop 1\n\t" \
  "v_min_i32_dpp v168, v168, v168 row_shr:1 row_mask:0xf bank_mask:0xf\n\t" \
  "s_nop 1\n\t" \
  "v_min_i32_dpp v168, v168, v168 row_shr:2 row_mask:0xf bank_mask:0xf\n\t" \
  "s_nop 1\n\t" \
  "v_min_i32_dpp v168, v168, v168 row_shr:4 row_mask:0xf bank_mask:0xf\n\t" \
  "s_nop 1\n\t" \
  "v_min_i32_dpp v168, v168, v168 row_shr:8 row_mask:0xf bank_mask:0xf\n\t" \
  "s_nop 1\n\t" \
  "v_readlane_b32 s53, v168, 15\n\t" \
  "s_add_u32 s57, s57, 1\n\t" \
  "s_cmp_ge_i32 s53, s52\n\t" \
  "s_cbranch_scc1 Lg_%=\n\t" \
  "s_sleep 2\n\t" \
  "s_cmp_lt_u32 s57, 65536\n\t" \
  "s_cbranch_scc1 Lp_%=\n\t" \
  "s_mov_b32 s53, 63\n\t" \
  "Lg_%=:\n\t" \
  "s_add_u32 s55, s52, 0\n\t" \
  "s_lshr_b32 s56, s55, 2\n\t" \
  "s_lshr_b32 s56, s49, s56\n\t" \
  "s_and_b32 s56, s56, 1\n\t" \
  "s_lshl_b32 s56, s56, 13\n\t" \
  "s_lshl_b32 s54, s55, 19\n\t" \
  "s_or_b32 s54, s54, s56\n\t" \
  "s_add_u32 s54, s54, s48\n\t" \
  "s_add_u32 s58, s54, 0\n\t" \
  "buffer_load_dwordx4 v[32:35], v16, s[36:39], s58 offen offset:0 sc1\n\t" \
  "buffer_load_dwordx4 v[36:39], v16, s[36:39], s58 offen offset:1024 sc1\n\t" \
  "buffer_load_dwordx4 v[40:43], v16, s[36:39], s58 offen offset:2048 sc1\n\t" \
  "buffer_load_dwordx4 v[44:47], v16, s[36:39], s58 offen offset:3072 sc1\n\t" \
  "s_add_u32 s58, s54, 4096\n\t" \
  "buffer_load_dwordx4 v[48:51], v16, s[36:39], s58 offen offset:0 sc1\n\t" \
  "buffer_load_dwordx4 v[52:55], v16, s[36:39], s58 offen offset:1024 sc1\n\t" \
  "buffer_load_dwordx4 v[56:59], v16, s[36:39], s58 offen offset:2048 sc1\n\t" \
  "buffer_load_dwordx4 v[60:63], v16, s[36:39], s58 offen offset:3072 sc1\n\t" \
  "s_add_u32 s58, s54, 16384\n\t" \
  "buffer_load_dwordx4 v[64:67], v16, s[36:39], s58 offen offset:0 sc1\n\t" \
  "buffer_load_dwordx4 v[68:71], v16, s[36:39], s58 offen offset:1024 sc1\n\t" \
  "buffer_load_dwordx4 v[72:75], v16, s[36:39], s58 offen offset:2048 sc1\n\t" \
  "buffer_load_dwordx4 v[76:79], v16, s[36:39], s58 offen offset:3072 sc1\n\t" \
  "s_add_u32 s58, s54, 20480\n\t" \
  "buffer_load_dwordx4 v[80:83], v16, s[36:39], s58 offen offset:0 sc1\n\t" \
  "buffer_load_dwordx4 v[84:87], v16, s[36:39], s58 offen offset:1024 sc1\n\t" \
  "buffer_load_dwordx4 v[88:91], v16, s[36:39], s58 offen offset:2048 sc1\n\t" \
  "buffer_load_dwordx4 v[92:95], v16, s[36:39], s58 offen offset:3072 sc1\n\t" \
  "Lt_%=:\n\t" \
  "s_add_u32 s58, s54, 32768\n\t" \
  "buffer_load_dwordx4 v[96:99], v16, s[36:39], s58 offen offset:0 sc1\n\t" \
  "buffer_load_dwordx4 v[100:103], v16, s[36:39], s58 offen offset:1024 sc1\n\t" \
  "buffer_load_dwordx4 v[104:107], v16, s[36:39], s58 offen offset:2048 sc1\n\t" \
  "buffer_load_dwordx4 v[108:111], v16, s[36:39], s58 offen offset:3072 sc1\n\t" \
  "s_add_u32 s58, s54, 36864\n\t" \
  "buffer_load_dwordx4 v[112:115], v16, s[36:39], s58 offen offset:0 sc1\n\t" \
  "buffer_load_dwordx4 v[116:119], v16, s[36:39], s58 offen offset:1024 sc1\n\t" \
  "buffer_load_dwordx4 v[120:123], v16, s[36:39], s58 offen offset:2048 sc1\n\t" \
  "buffer_load_dwordx4 v[124:127], v16, s[36:39], s58 offen offset:3072 sc1\n\t" \
  "s_add_u32 s58, s54, 49152\n\t" \
  "buffer_load_dwordx4 v[128:131], v16, s[36:39], s58 offen offset:0 sc1\n\t" \
  "buffer_load_dwordx4 v[132:135], v16, s[36:39], s58 offen offset:1024 sc1\n\t" \
  "buffer_load_dwordx4 v[136:139], v16, s[36:39], s58 offen offset:2048 sc1\n\t" \
  "buffer_load_dwordx4 v[140:143], v16, s[36:39], s58 offen offset:3072 sc1\n\t" \
  "s_add_u32 s58, s54, 53248\n\t" \
  "buffer_load_dwordx4 v[144:147], v16, s[36:39], s58 offen offset:0 sc1\n\t" \
  "buffer_load_dwordx4 v[148:151], v16, s[36:39], s58 offen offset:1024 sc1\n\t" \
  "buffer_load_dwordx4 v[152:155], v16, s[36:39], s58 offen offset:2048 sc1\n\t" \
  "buffer_load_dwordx4 v[156:159], v16, s[36:39], s58 offen offset:3072 sc1\n\t" \
  "s_waitcnt vmcnt(30)\n\t" \
  "v_mfma_f32_16x16x32_bf16 v[160:163], a[0:3], v[32:35], 0\n\t" \
  "v_mfma_f32_16x16x32_bf16 v[164:167], a[4:7], v[32:35], 0\n\t" \
  "v_mfma_f32_16x16x32_bf16 v[160:163], a[8:11], v[36:39], v[160:163]\n\t" \
  "v_mfma_f32_16x16x32_bf16 v[164:167], a[12:15], v[36:39], v[164:167]\n\t" \
  "s_waitcnt vmcnt(28)\n\t" \
  "v_mfma_f32_16x16x32_bf16 v[160:163], a[16:19], v[40:43], v[160:163]\n\t" \
  "v_mfma_f32_16x16x32_bf16 v[164:167], a[20:23], v[40:43], v[164:167]\n\t" \
  "v_mfma_f32_16x16x32_bf16 v[160:163], a[24:27], v[44:47], v[160:163]\n\t" \
  "v_mfma_f32_16x16x32_bf16 v[164:167], a[28:31], v[44:47], v[164:167]\n\t" \
  "s_waitcnt vmcnt(26)\n\t" \
  "v_mfma_f32_16x16x32_bf16 v[160:163], a[32:35], v[48:51], v[160:163]\n\t" \
  "v_mfma_f32_16x16x32_bf16 v[164:167], a[36:39], v[48:51], v[164:167]\n\t" \
  "v_mfma_f32_16x16x32_bf16 v[160:163], a[40:43], v[52:55], v[160:163]\n\t" \
  "v_mfma_f32_16x16x32_bf16 v[164:167], a[44:47], v[52:55], v[164:167]\n\t" \
  "s_waitcnt vmcnt(24)\n\t" \
  "v_mfma_f32_16x16x32_bf16 v[160:163], a[48:51], v[56:59], v[160:163]\n\t" \
  "v_mfma_f32_16x16x32_bf16 v[164:167], a[52:55], v[56:59], v[164:167]\n\t" \
  "v_mfma_f32_16x16x32_bf16 v[160:163], a[56:59], v[60:63], v[160:163]\n\t" \
  "v_mfma_f32_16x16x32_bf16 v[164:167], a[60:63], v[60:63], v[164:167]\n\t" \
  "s_waitcnt vmcnt(22)\n\t" \
  "v_mfma_f32_16x16x32_bf16 v[160:163], a[64:67], v[64:67], v[160:163]\n\t" \
  "v_mfma_f32_16x16x32_bf16 v[164:167], a[68:71], v[64:67], v[164:167]\n\t" \
  "v_mfma_f32_16x16x32_bf16 v[160:163], a[72:75], v[68:71], v[160:163]\n\t" \
  "v_mfma_f32_16x16x32_bf16 v[164:167], a[76:79], v[68:71], v[164:167]\n\t" \
  "s_waitcnt vmcnt(20)\n\t" \
  "v_mfma_f32_16x16x32_bf16 v[160:163], a[80:83], v[72:75], v[160:163]\n\t" \
  "v_mfma_f32_16x16x32_bf16 v[164:167], a[84:87], v[72:75], v[164:167]\n\t" \
  "v_mfma_f32_16x16x32_bf16 v[160:163], a[88:91], v[76:79], v[160:163]\n\t" \
  "v_mfma_f32_16x16x32_bf16 v[164:167], a[92:95], v[76:79], v[164:167]\n\t" \
  "s_waitcnt vmcnt(18)\n\t" \
  "v_mfma_f32_16x16x32_bf16 v[160:163], a[96:99], v[80:83], v[160:163]\n\t" \
  "v_mfma_f32_16x16x32_bf16 v[164:167], a[100:103], v[80:83], v[164:167]\n\t" \
  "v_mfma_f32_16x16x32_bf16 v[160:163], a[104:107], v[84:87], v[160:163]\n\t" \
  "v_mfma_f32_16x16x32_bf16 v[164:167], a[108:111], v[84:87], v[164:167]\n\t" \
  "s_waitcnt vmcnt(16)\n\t" \
  "v_mfma_f32_16x16x32_bf16 v[160:163], a[112:115], v[88:91], v[160:163]\n\t" \
  "v_mfma_f32_16x16x32_bf16 v[164:167], a[116:119], v[88:91], v[164:167]\n\t" \
  "v_mfma_f32_16x16x32_bf16 v[160:163], a[120:123], v[92:95], v[160:163]\n\t" \
  "v_mfma_f32_16x16x32_bf16 v[164:167], a[124:127], v[92:95], v[164:167]\n\t" \
  "s_cmp_gt_i32 s53, s52\n\t" \
  "s_cbranch_scc0 Ln_%=\n\t" \
  "s_add_u32 s55, s52, 1\n\t" \
  "s_lshr_b32 s56, s55, 2\n\t" \
  "s_lshr_b32 s56, s49, s56\n\t" \
  "s_and_b32 s56, s56, 1\n\t" \
  "s_lshl_b32 s56, s56, 13\n\t" \
  "s_lshl_b32 s54, s55, 19\n\t" \
  "s_or_b32 s54, s54, s56\n\t" \
  "s_add_u32 s54, s54, s48\n\t" \
  "s_add_u32 s58, s54, 0\n\t" \
  "buffer_load_dwordx4 v[32:35], v16, s[36:39], s58 offen offset:0 sc1\n\t" \
  "buffer_load_dwordx4 v[36:39], v16, s[36:39], s58 offen offset:1024 sc1\n\t" \
  "buffer_load_dwordx4 v[40:43], v16, s[36:39], s58 offen offset:2048 sc1\n\t" \
  "buffer_load_dwordx4 v[44:47], v16, s[36:39], s58 offen offset:3072 sc1\n\t" \
  "s_add_u32 s58, s54, 4096\n\t" \
  "buffer_load_dwordx4 v[48:51], v16, s[36:39], s58 offen offset:0 sc1\n\t" \
  "buffer_load_dwordx4 v[52:55], v16, s[36:39], s58 offen offset:1024 sc1\n\t" \
  "buffer_load_dwordx4 v[56:59], v16, s[36:39], s58 offen offset:2048 sc1\n\t" \
  "buffer_load_dwordx4 v[60:63], v16, s[36:39], s58 offen offset:3072 sc1\n\t" \
  "s_add_u32 s58, s54, 16384\n\t" \
  "buffer_load_dwordx4 v[64:67], v16, s[36:39], s58 offen offset:0 sc1\n\t" \
  "buffer_load_dwordx4 v[68:71], v16, s[36:39], s58 offen offset:1024 sc1\n\t" \
  "buffer_load_dwordx4 v[72:75], v16, s[36:39], s58 offen offset:2048 sc1\n\t" \
  "buffer_load_dwordx4 v[76:79], v16, s[36:39], s58 offen offset:3072 sc1\n\t" \
  "s_add_u32 s58, s54, 20480\n\t" \
  "buffer_load_dwordx4 v[80:83], v16, s[36:39], s58 offen offset:0 sc1\n\t" \
  "buffer_load_dwordx4 v[84:87], v16, s[36:39], s58 offen offset:1024 sc1\n\t" \
  "buffer_load_dwordx4 v[88:91], v16, s[36:39], s58 offen offset:2048 sc1\n\t" \
  "buffer_load_dwordx4 v[92:95], v16, s[36:39], s58 offen offset:3072 sc1\n\t" \
  "s_waitcnt vmcnt(30)\n\t" \
  "v_mfma_f32_16x16x32_bf16 v[160:163], a[128:131], v[96:99], v[160:163]\n\t" \
  "v_mfma_f32_16x16x32_bf16 v[164:167], a[132:135], v[96:99], v[164:167]\n\t" \
  "v_mfma_f32_16x16x32_bf16 v[160:163], a[136:139], v[100:103], v[160:163]\n\t" \
  "v_mfma_f32_16x16x32_bf16 v[164:167], a[140:143], v[100:103], v[164:167]\n\t" \
  "s_waitcnt vmcnt(28)\n\t" \
  "v_mfma_f32_16x16x32_bf16 v[160:163], a[144:147], v[104:107], v[160:163]\n\t" \
  "v_mfma_f32_16x16x32_bf16 v[164:167], a[148:151], v[104:107], v[164:167]\n\t" \
  "v_mfma_f32_16x16x32_bf16 v[160:163], a[152:155], v[108:111], v[160:163]\n\t" \
  "v_mfma_f32_16x16x32_bf16 v[164:167], a[156:159], v[108:111], v[164:167]\n\t" \
  "s_waitcnt vmcnt(26)\n\t" \
  "v_mfma_f32_16x16x32_bf16 v[160:163], a[160:163], v[112:115], v[160:163]\n\t" \
  "v_mfma_f32_16x16x32_bf16 v[164:167], a[164:167], v[112:115], v[164:167]\n\t" \
  "v_mfma_f32_16x16x32_bf16 v[160:163], a[168:171], v[116:119], v[160:163]\n\t" \
  "v_mfma_f32_16x16x32_bf16 v[164:167], a[172:175], v[116:119], v[164:167]\n\t" \
  "s_waitcnt vmcnt(24)\n\t" \
  "v_mfma_f32_16x16x32_bf16 v[160:163], a[176:179], v[120:123], v[160:163]\n\t" \
  "v_mfma_f32_16x16x32_bf16 v[164:167], a[180:183], v[120:123], v[164:167]\n\t" \
  "v_mfma_f32_16x16x32_bf16 v[160:163], a[184:187], v[124:127], v[160:163]\n\t" \
  "v_mfma_f32_16x16x32_bf16 v[164:167], a[188:191], v[124:127], v[164:167]\n\t" \
  "s_waitcnt vmcnt(22)\n\t" \
  "v_mfma_f32_16x16x32_bf16 v[160:163], a[192:195], v[128:131], v[160:163]\n\t" \
  "v_mfma_f32_16x16x32_bf16 v[164:167], a[196:199], v[128:131], v[164:167]\n\t" \
  "v_mfma_f32_16x16x32_bf16 v[160:163], a[200:203], v[132:135], v[160:163]\n\t" \
  "v_mfma_f32_16x16x32_bf16 v[164:167], a[204:207], v[132:135], v[164:167]\n\t" \
  "s_waitcnt vmcnt(20)\n\t" \
  "v_mfma_f32_16x16x32_bf16 v[160:163], a[208:211], v[136:139], v[160:163]\n\t" \
  "v_mfma_f32_16x16x32_bf16 v[164:167], a[212:215], v[136:139], v[164:167]\n\t" \
  "v_mfma_f32_16x16x32_bf16 v[160:163], a[216:219], v[140:143], v[160:163]\n\t" \
  "v_mfma_f32_16x16x32_bf16 v[164:167], a[220:223], v[140:143], v[164:167]\n\t" \
  "s_waitcnt vmcnt(18)\n\t" \
  "v_mfma_f32_16x16x32_bf16 v[160:163], a[224:227], v[144:147], v[160:163]\n\t" \
  "v_mfma_f32_16x16x32_bf16 v[164:167], a[228:231], v[144:147], v[164:167]\n\t" \
  "v_mfma_f32_16x16x32_bf16 v[160:163], a[232:235], v[148:151], v[160:163]\n\t" \
  "v_mfma_f32_16x16x32_bf16 v[164:167], a[236:239], v[148:151], v[164:167]\n\t" \
  "s_waitcnt vmcnt(16)\n\t" \
  "v_mfma_f32_16x16x32_bf16 v[160:163], a[240:243], v[152:155], v[160:163]\n\t" \
  "v_mfma_f32_16x16x32_bf16 v[164:167], a[244:247], v[152:155], v[164:167]\n\t" \
  "v_mfma_f32_16x16x32_bf16 v[160:163], a[248:251], v[156:159], v[160:163]\n\t" \
  "v_mfma_f32_16x16x32_bf16 v[164:167], a[252:255], v[156:159], v[164:167]\n\t" \
  "s_mov_b32 s57, 1\n\t" \
  "s_branch Lr_%=\n\t" \
  "Ln_%=:\n\t" \
  "s_waitcnt vmcnt(14)\n\t" \
  "v_mfma_f32_16x16x32_bf16 v[160:163], a[128:131], v[96:99], v[160:163]\n\t" \
  "v_mfma_f32_16x16x32_bf16 v[164:167], a[132:135], v[96:99], v[164:167]\n\t" \
  "v_mfma_f32_16x16x32_bf16 v[160:163], a[136:139], v[100:103], v[160:163]\n\t" \
  "v_mfma_f32_16x16x32_bf16 v[164:167], a[140:143], v[100:103], v[164:167]\n\t" \
  "s_waitcnt vmcnt(12)\n\t" \
  "v_mfma_f32_16x16x32_bf16 v[160:163], a[144:147], v[104:107], v[160:163]\n\t" \
  "v_mfma_f32_16x16x32_bf16 v[164:167], a[148:151], v[104:107], v[164:167]\n\t" \
  "v_mfma_f32_16x16x32_bf16 v[160:163], a[152:155], v[108:111], v[160:163]\n\t" \
  "v_mfma_f32_16x16x32_bf16 v[164:167], a[156:159], v[108:111], v[164:167]\n\t" \
  "s_waitcnt vmcnt(10)\n\t" \
  "v_mfma_f32_16x16x32_bf16 v[160:163], a[160:163], v[112:115], v[160:163]\n\t" \
  "v_mfma_f32_16x16x32_bf16 v[164:167], a[164:167], v[112:115], v[164:167]\n\t" \
  "v_mfma_f32_16x16x32_bf16 v[160:163], a[168:171], v[116:119], v[160:163]\n\t" \
  "v_mfma_f32_16x16x32_bf16 v[164:167], a[172:175], v[116:119], v[164:167]\n\t" \
  "s_waitcnt vmcnt(8)\n\t" \
  "v_mfma_f32_16x16x32_bf16 v[160:163], a[176:179], v[120:123], v[160:163]\n\t" \
  "v_mfma_f32_16x16x32_bf16 v[164:167], a[180:183], v[120:123], v[164:167]\n\t" \
  "v_mfma_f32_16x16x32_bf16 v[160:163], a[184:187], v[124:127], v[160:163]\n\t" \
  "v_mfma_f32_16x16x32_bf16 v[164:167], a[188:191], v[124:127], v[164:167]\n\t" \
  "s_waitcnt vmcnt(6)\n\t" \
  "v_mfma_f32_16x16x32_bf16 v[160:163], a[192:195], v[128:131], v[160:163]\n\t" \
  "v_mfma_f32_16x16x32_bf16 v[164:167], a[196:199], v[128:131], v[164:167]\n\t" \
  "v_mfma_f32_16x16x32_bf16 v[160:163], a[200:203], v[132:135], v[160:163]\n\t" \
  "v_mfma_f32_16x16x32_bf16 v[164:167], a[204:207], v[132:135], v[164:167]\n\t" \
  "s_waitcnt vmcnt(4)\n\t" \
  "v_mfma_f32_16x16x32_bf16 v[160:163], a[208:211], v[136:139], v[160:163]\n\t" \
  "v_mfma_f32_16x16x32_bf16 v[164:167], a[212:215], v[136:139], v[164:167]\n\t" \
  "v_mfma_f32_16x16x32_bf16 v[160:163], a[216:219], v[140:143], v[160:163]\n\t" \
  "v_mfma_f32_16x16x32_bf16 v[164:167], a[220:223], v[140:143], v[164:167]\n\t" \
  "s_waitcnt vmcnt(2)\n\t" \
  "v_mfma_f32_16x16x32_bf16 v[160:163], a[224:227], v[144:147], v[160:163]\n\t" \
  "v_mfma_f32_16x16x32_bf16 v[164:167], a[228:231], v[144:147], v[164:167]\n\t" \
  "v_mfma_f32_16x16x32_bf16 v[160:163], a[232:235], v[148:151], v[160:163]\n\t" \
  "v_mfma_f32_16x16x32_bf16 v[164:167], a[236:239], v[148:151], v[164:167]\n\t" \
  "s_waitcnt vmcnt(0)\n\t" \
  "v_mfma_f32_16x16x32_bf16 v[160:163], a[240:243], v[152:155], v[160:163]\n\t" \
  "v_mfma_f32_16x16x32_bf16 v[164:167], a[244:247], v[152:155], v[164:167]\n\t" \
  "v_mfma_f32_16x16x32_bf16 v[160:163], a[248:251], v[156:159], v[160:163]\n\t" \
  "v_mfma_f32_16x16x32_bf16 v[164:167], a[252:255], v[156:159], v[164:167]\n\t" \
  "s_mov_b32 s57, 0\n\t" \
  "Lr_%=:\n\t" \
  "v_xor_b32 v19, 0x2000, v19\n\t" \
  "v_xor_b32 v20, 0x2000, v20\n\t" \
  "s_nop 4\n\t" \
  "ds_write_b128 v19, v[160:163]\n\t" \
  "ds_write_b128 v19, v[164:167] offset:1024\n\t" \
  "s_cmp_lt_u32 s51, 2\n\t" \
  "s_waitcnt lgkmcnt(0)\n\t" \
  "s_barrier\n\t" \
  "s_cbranch_scc0 Ls_%=\n\t" \
  "ds_read_b128 v[168:171], v20 offset:0\n\t" \
  "ds_read_b128 v[172:175], v20 offset:2048\n\t" \
  "ds_read_b128 v[176:179], v20 offset:4096\n\t" \
  "ds_read_b128 v[180:183], v20 offset:6144\n\t" \
  "s_lshl_b32 s55, s52, 16\n\t" \
  "s_add_u32 s55, s55, s50\n\t" \
  "s_waitcnt lgkmcnt(2)\n\t" \
  "v_add_f32 v168, v168, v172\n\t" \
  "v_add_f32 v169, v169, v173\n\t" \
  "v_add_f32 v170, v170, v174\n\t" \
  "v_add_f32 v171, v171, v175\n\t" \
  "s_waitcnt lgkmcnt(0)\n\t" \
  "v_add_f32 v176, v176, v180\n\t" \
  "v_add_f32 v177, v177, v181\n\t" \
  "v_add_f32 v178, v178, v182\n\t" \
  "v_add_f32 v179, v179, v183\n\t" \
  "v_add_f32 v168, v168, v176\n\t" \
  "v_add_f32 v169, v169, v177\n\t" \
  "v_add_f32 v170, v170, v178\n\t" \
  "v_add_f32 v171, v171, v179\n\t" \
  "v_cvt_pk_bf16_f32 v168, v168, v169\n\t" \
  "v_cvt_pk_bf16_f32 v169, v170, v171\n\t" \
  "buffer_store_dwordx2 v[168:169], v21, s[40:43], s55 offen\n\t" \
  "s_branch Lu_%=\n\t" \
  "Ls_%=:\n\t" \
  "buffer_store_dwordx2 v[168:169], v21, s[40:43], s50 offen\n\t" \
  "Lu_%=:\n\t" \
  "s_add_u32 s52, s52, 1\n\t" \
  "s_cmp_eq_u32 s52, 64\n\t" \
  "s_cbranch_scc1 Le_%=\n\t" \
  "s_cmp_eq_u32 s57, 0\n\t" \
  "s_cbranch_scc1 Lw_%=\n\t" \
  "s_branch Lt2_%=\n\t" \
  "Lt2_%=:\n\t" \
  "s_add_u32 s58, s54, 32768\n\t" \
  "buffer_load_dwordx4 v[96:99], v16, s[36:39], s58 offen offset:0 sc1\n\t" \
  "buffer_load_dwordx4 v[100:103], v16, s[36:39], s58 offen offset:1024 sc1\n\t" \
  "buffer_load_dwordx4 v[104:107], v16, s[36:39], s58 offen offset:2048 sc1\n\t" \
  "buffer_load_dwordx4 v[108:111], v16, s[36:39], s58 offen offset:3072 sc1\n\t" \
  "s_add_u32 s58, s54, 36864\n\t" \
  "buffer_load_dwordx4 v[112:115], v16, s[36:39], s58 offen offset:0 sc1\n\t" \
  "buffer_load_dwordx4 v[116:119], v16, s[36:39], s58 offen offset:1024 sc1\n\t" \
  "buffer_load_dwordx4 v[120:123], v16, s[36:39], s58 offen offset:2048 sc1\n\t" \
  "buffer_load_dwordx4 v[124:127], v16, s[36:39], s58 offen offset:3072 sc1\n\t" \
  "s_add_u32 s58, s54, 49152\n\t" \
  "buffer_load_dwordx4 v[128:131], v16, s[36:39], s58 offen offset:0 sc1\n\t" \
  "buffer_load_dwordx4 v[132:135], v16, s[36:39], s58 offen offset:1024 sc1\n\t" \
  "buffer_load_dwordx4 v[136:139], v16, s[36:39], s58 offen offset:2048 sc1\n\t" \
  "buffer_load_dwordx4 v[140:143], v16, s[36:39], s58 offen offset:3072 sc1\n\t" \
  "s_add_u32 s58, s54, 53248\n\t" \
  "buffer_load_dwordx4 v[144:147], v16, s[36:39], s58 offen offset:0 sc1\n\t" \
  "buffer_load_dwordx4 v[148:151], v16, s[36:39], s58 offen offset:1024 sc1\n\t" \
  "buffer_load_dwordx4 v[152:155], v16, s[36:39], s58 offen offset:2048 sc1\n\t" \
  "buffer_load_dwordx4 v[156:159], v16, s[36:39], s58 offen offset:3072 sc1\n\t" \
  "s_waitcnt vmcnt(31)\n\t" \
  "v_mfma_f32_16x16x32_bf16 v[160:163], a[0:3], v[32:35], 0\n\t" \
  "v_mfma_f32_16x16x32_bf16 v[164:167], a[4:7], v[32:35], 0\n\t" \
  "v_mfma_f32_16x16x32_bf16 v[160:163], a[8:11], v[36:39], v[160:163]\n\t" \
  "v_mfma_f32_16x16x32_bf16 v[164:167], a[12:15], v[36:39], v[164:167]\n\t" \
  "s_waitcnt vmcnt(29)\n\t" \
  "v_mfma_f32_16x16x32_bf16 v[160:163], a[16:19], v[40:43], v[160:163]\n\t" \
  "v_mfma_f32_16x16x32_bf16 v[164:167], a[20:23], v[40:43], v[164:167]\n\t" \
  "v_mfma_f32_16x16x32_bf16 v[160:163], a[24:27], v[44:47], v[160:163]\n\t" \
  "v_mfma_f32_16x16x32_bf16 v[164:167], a[28:31], v[44:47], v[164:167]\n\t" \
  "s_waitcnt vmcnt(27)\n\t" \
  "v_mfma_f32_16x16x32_bf16 v[160:163], a[32:35], v[48:51], v[160:163]\n\t" \
  "v_mfma_f32_16x16x32_bf16 v[164:167], a[36:39], v[48:51], v[164:167]\n\t" \
  "v_mfma_f32_16x16x32_bf16 v[160:163], a[40:43], v[52:55], v[160:163]\n\t" \
  "v_mfma_f32_16x16x32_bf16 v[164:167], a[44:47], v[52:55], v[164:167]\n\t" \
  "s_waitcnt vmcnt(25)\n\t" \
  "v_mfma_f32_16x16x32_bf16 v[160:163], a[48:51], v[56:59], v[160:163]\n\t" \
  "v_mfma_f32_16x16x32_bf16 v[164:167], a[52:55], v[56:59], v[164:167]\n\t" \
  "v_mfma_f32_16x16x32_bf16 v[160:163], a[56:59], v[60:63], v[160:163]\n\t" \
  "v_mfma_f32_16x16x32_bf16 v[164:167], a[60:63], v[60:63], v[164:167]\n\t" \
  "s_waitcnt vmcnt(23)\n\t" \
  "v_mfma_f32_16x16x32_bf16 v[160:163], a[64:67], v[64:67], v[160:163]\n\t" \
  "v_mfma_f32_16x16x32_bf16 v[164:167], a[68:71], v[64:67], v[164:167]\n\t" \
  "v_mfma_f32_16x16x32_bf16 v[160:163], a[72:75], v[68:71], v[160:163]\n\t" \
  "v_mfma_f32_16x16x32_bf16 v[164:167], a[76:79], v[68:71], v[164:167]\n\t" \
  "s_waitcnt vmcnt(21)\n\t" \
  "v_mfma_f32_16x16x32_bf16 v[160:163], a[80:83], v[72:75], v[160:163]\n\t" \
  "v_mfma_f32_16x16x32_bf16 v[164:167], a[84:87], v[72:75], v[164:167]\n\t" \
  "v_mfma_f32_16x16x32_bf16 v[160:163], a[88:91], v[76:79], v[160:163]\n\t" \
  "v_mfma_f32_16x16x32_bf16 v[164:167], a[92:95], v[76:79], v[164:167]\n\t" \
  "s_waitcnt vmcnt(19)\n\t" \
  "v_mfma_f32_16x16x32_bf16 v[160:163], a[96:99], v[80:83], v[160:163]\n\t" \
  "v_mfma_f32_16x16x32_bf16 v[164:167], a[100:103], v[80:83], v[164:167]\n\t" \
  "v_mfma_f32_16x16x32_bf16 v[160:163], a[104:107], v[84:87], v[160:163]\n\t" \
  "v_mfma_f32_16x16x32_bf16 v[164:167], a[108:111], v[84:87], v[164:167]\n\t" \
  "s_waitcnt vmcnt(17)\n\t" \
  "v_mfma_f32_16x16x32_bf16 v[160:163], a[112:115], v[88:91], v[160:163]\n\t" \
  "v_mfma_f32_16x16x32_bf16 v[164:167], a[116:119], v[88:91], v[164:167]\n\t" \
  "v_mfma_f32_16x16x32_bf16 v[160:163], a[120:123], v[92:95], v[160:163]\n\t" \
  "v_mfma_f32_16x16x32_bf16 v[164:167], a[124:127], v[92:95], v[164:167]\n\t" \
  "s_cmp_gt_i32 s53, s52\n\t" \
  "s_cbranch_scc0 Ln_%=\n\t" \
  "s_add_u32 s55, s52, 1\n\t" \
  "s_lshr_b32 s56, s55, 2\n\t" \
  "s_lshr_b32 s56, s49, s56\n\t" \
  "s_and_b32 s56, s56, 1\n\t" \
  "s_lshl_b32 s56, s56, 13\n\t" \
  "s_lshl_b32 s54, s55, 19\n\t" \
  "s_or_b32 s54, s54, s56\n\t" \
  "s_add_u32 s54, s54, s48\n\t" \
  "s_add_u32 s58, s54, 0\n\t" \
  "buffer_load_dwordx4 v[32:35], v16, s[36:39], s58 offen offset:0 sc1\n\t" \
  "buffer_load_dwordx4 v[36:39], v16, s[36:39], s58 offen offset:1024 sc1\n\t" \
  "buffer_load_dwordx4 v[40:43], v16, s[36:39], s58 offen offset:2048 sc1\n\t" \
  "buffer_load_dwordx4 v[44:47], v16, s[36:39], s58 offen offset:3072 sc1\n\t" \
  "s_add_u32 s58, s54, 4096\n\t" \
  "buffer_load_dwordx4 v[48:51], v16, s[36:39], s58 offen offset:0 sc1\n\t" \
  "buffer_load_dwordx4 v[52:55], v16, s[36:39], s58 offen offset:1024 sc1\n\t" \
  "buffer_load_dwordx4 v[56:59], v16, s[36:39], s58 offen offset:2048 sc1\n\t" \
  "buffer_load_dwordx4 v[60:63], v16, s[36:39], s58 offen offset:3072 sc1\n\t" \
  "s_add_u32 s58, s54, 16384\n\t" \
  "buffer_load_dwordx4 v[64:67], v16, s[36:39], s58 offen offset:0 sc1\n\t" \
  "buffer_load_dwordx4 v[68:71], v16, s[36:39], s58 offen offset:1024 sc1\n\t" \
  "buffer_load_dwordx4 v[72:75], v16, s[36:39], s58 offen offset:2048 sc1\n\t" \
  "buffer_load_dwordx4 v[76:79], v16, s[36:39], s58 offen offset:3072 sc1\n\t" \
  "s_add_u32 s58, s54, 20480\n\t" \
  "buffer_load_dwordx4 v[80:83], v16, s[36:39], s58 offen offset:0 sc1\n\t" \
  "buffer_load_dwordx4 v[84:87], v16, s[36:39], s58 offen offset:1024 sc1\n\t" \
  "buffer_load_dwordx4 v[88:91], v16, s[36:39], s58 offen offset:2048 sc1\n\t" \
  "buffer_load_dwordx4 v[92:95], v16, s[36:39], s58 offen offset:3072 sc1\n\t" \
  "s_waitcnt vmcnt(30)\n\t" \
  "v_mfma_f32_16x16x32_bf16 v[160:163], a[128:131], v[96:99], v[160:163]\n\t" \
  "v_mfma_f32_16x16x32_bf16 v[164:167], a[132:135], v[96:99], v[164:167]\n\t" \
  "v_mfma_f32_16x16x32_bf16 v[160:163], a[136:139], v[100:103], v[160:163]\n\t" \
  "v_mfma_f32_16x16x32_bf16 v[164:167], a[140:143], v[100:103], v[164:167]\n\t" \
  "s_waitcnt vmcnt(28)\n\t" \
  "v_mfma_f32_16x16x32_bf16 v[160:163], a[144:147], v[104:107], v[160:163]\n\t" \
  "v_mfma_f32_16x16x32_bf16 v[164:167], a[148:151], v[104:107], v[164:167]\n\t" \
  "v_mfma_f32_16x16x32_bf16 v[160:163], a[152:155], v[108:111], v[160:163]\n\t" \
  "v_mfma_f32_16x16x32_bf16 v[164:167], a[156:159], v[108:111], v[164:167]\n\t" \
  "s_waitcnt vmcnt(26)\n\t" \
  "v_mfma_f32_16x16x32_bf16 v[160:163], a[160:163], v[112:115], v[160:163]\n\t" \
  "v_mfma_f32_16x16x32_bf16 v[164:167], a[164:167], v[112:115], v[164:167]\n\t" \
  "v_mfma_f32_16x16x32_bf16 v[160:163], a[168:171], v[116:119], v[160:163]\n\t" \
  "v_mfma_f32_16x16x32_bf16 v[164:167], a[172:175], v[116:119], v[164:167]\n\t" \
  "s_waitcnt vmcnt(24)\n\t" \
  "v_mfma_f32_16x16x32_bf16 v[160:163], a[176:179], v[120:123], v[160:163]\n\t" \
  "v_mfma_f32_16x16x32_bf16 v[164:167], a[180:183], v[120:123], v[164:167]\n\t" \
  "v_mfma_f32_16x16x32_bf16 v[160:163], a[184:187], v[124:127], v[160:163]\n\t" \
  "v_mfma_f32_16x16x32_bf16 v[164:167], a[188:191], v[124:127], v[164:167]\n\t" \
  "s_waitcnt vmcnt(22)\n\t" \
  "v_mfma_f32_16x16x32_bf16 v[160:163], a[192:195], v[128:131], v[160:163]\n\t" \
  "v_mfma_f32_16x16x32_bf16 v[164:167], a[196:199], v[128:131], v[164:167]\n\t" \
  "v_mfma_f32_16x16x32_bf16 v[160:163], a[200:203], v[132:135], v[160:163]\n\t" \
  "v_mfma_f32_16x16x32_bf16 v[164:167], a[204:207], v[132:135], v[164:167]\n\t" \
  "s_waitcnt vmcnt(20)\n\t" \
  "v_mfma_f32_16x16x32_bf16 v[160:163], a[208:211], v[136:139], v[160:163]\n\t" \
  "v_mfma_f32_16x16x32_bf16 v[164:167], a[212:215], v[136:139], v[164:167]\n\t" \
  "v_mfma_f32_16x16x32_bf16 v[160:163], a[216:219], v[140:143], v[160:163]\n\t" \
  "v_mfma_f32_16x16x32_bf16 v[164:167], a[220:223], v[140:143], v[164:167]\n\t" \
  "s_waitcnt vmcnt(18)\n\t" \
  "v_mfma_f32_16x16x32_bf16 v[160:163], a[224:227], v[144:147], v[160:163]\n\t" \
  "v_mfma_f32_16x16x32_bf16 v[164:167], a[228:231], v[144:147], v[164:167]\n\t" \
  "v_mfma_f32_16x16x32_bf16 v[160:163], a[232:235], v[148:151], v[160:163]\n\t" \
  "v_mfma_f32_16x16x32_bf16 v[164:167], a[236:239], v[148:151], v[164:167]\n\t" \
  "s_waitcnt vmcnt(16)\n\t" \
  "v_mfma_f32_16x16x32_bf16 v[160:163], a[240:243], v[152:155], v[160:163]\n\t" \
  "v_mfma_f32_16x16x32_bf16 v[164:167], a[244:247], v[152:155], v[164:167]\n\t" \
  "v_mfma_f32_16x16x32_bf16 v[160:163], a[248:251], v[156:159], v[160:163]\n\t" \
  "v_mfma_f32_16x16x32_bf16 v[164:167], a[252:255], v[156:159], v[164:167]\n\t" \
  "s_mov_b32 s57, 1\n\t" \
  "s_branch Lr_%=\n\t" \
  "Le_%=:\n\t"
#define OPROJ_CLOBBERS "a0", "a1", "a2", "a3", "a4", "a5", "a6", "a7", "a8", "a9", "a10", "a11", "a12", "a13", "a14", "a15", "a16", "a17", "a18", "a19", "a20", "a21", "a22", "a23", "a24", "a25", "a26", "a27", "a28", "a29", "a30", "a31", "a32", "a33", "a34", "a35", "a36", "a37", "a38", "a39", "a40", "a41", "a42", "a43", "a44", "a45", "a46", "a47", "a48", "a49", "a50", "a51", "a52", "a53", "a54", "a55", "a56", "a57", "a58", "a59", "a60", "a61", "a62", "a63", "a64", "a65", "a66", "a67", "a68", "a69", "a70", "a71", "a72", "a73", "a74", "a75", "a76", "a77", "a78", "a79", "a80", "a81", "a82", "a83", "a84", "a85", "a86", "a87", "a88", "a89", "a90", "a91", "a92", "a93", "a94", "a95", "a96", "a97", "a98", "a99", "a100", "a101", "a102", "a103", "a104", "a105", "a106", "a107", "a108", "a109", "a110", "a111", "a112", "a113", "a114", "a115", "a116", "a117", "a118", "a119", "a120", "a121", "a122", "a123", "a124", "a125", "a126", "a127", "a128", "a129", "a130", "a131", "a132", "a133", "a134", "a135", "a136", "a137", "a138", "a139", "a140", "a141", "a142", "a143", "a144", "a145", "a146", "a147", "a148", "a149", "a150", "a151", "a152", "a153", "a154", "a155", "a156", "a157", "a158", "a159", "a160", "a161", "a162", "a163", "a164", "a165", "a166", "a167", "a168", "a169", "a170", "a171", "a172", "a173", "a174", "a175", "a176", "a177", "a178", "a179", "a180", "a181", "a182", "a183", "a184", "a185", "a186", "a187", "a188", "a189", "a190", "a191", "a192", "a193", "a194", "a195", "a196", "a197", "a198", "a199", "a200", "a201", "a202", "a203", "a204", "a205", "a206", "a207", "a208", "a209", "a210", "a211", "a212", "a213", "a214", "a215", "a216", "a217", "a218", "a219", "a220", "a221", "a222", "a223", "a224", "a225", "a226", "a227", "a228", "a229", "a230", "a231", "a232", "a233", "a234", "a235", "a236", "a237", "a238", "a239", "a240", "a241", "a242", "a243", "a244", "a245", "a246", "a247", "a248", "a249", "a250", "a251", "a252", "a253", "a254", "a255", "v32", "v33", "v34", "v35", "v36", "v37", "v38", "v39", "v40", "v41", "v42", "v43", "v44", "v45", "v46", "v47", "v48", "v49", "v50", "v51", "v52", "v53", "v54", "v55", "v56", "v57", "v58", "v59", "v60", "v61", "v62", "v63", "v64", "v65", "v66", "v67", "v68", "v69", "v70", "v71", "v72", "v73", "v74", "v75", "v76", "v77", "v78", "v79", "v80", "v81", "v82", "v83", "v84", "v85", "v86", "v87", "v88", "v89", "v90", "v91", "v92", "v93", "v94", "v95", "v96", "v97", "v98", "v99", "v100", "v101", "v102", "v103", "v104", "v105", "v106", "v107", "v108", "v109", "v110", "v111", "v112", "v113", "v114", "v115", "v116", "v117", "v118", "v119", "v120", "v121", "v122", "v123", "v124", "v125", "v126", "v127", "v128", "v129", "v130", "v131", "v132", "v133", "v134", "v135", "v136", "v137", "v138", "v139", "v140", "v141", "v142", "v143", "v144", "v145", "v146", "v147", "v148", "v149", "v150", "v151", "v152", "v153", "v154", "v155", "v156", "v157", "v158", "v159", "v160", "v161", "v162", "v163", "v164", "v165", "v166", "v167", "v168", "v169", "v170", "v171", "v172", "v173", "v174", "v175", "v176", "v177", "v178", "v179", "v180", "v181", "v182", "v183", "s52", "s53", "s54", "s55", "s56", "s57", "s58", "s59", "vcc", "scc", "memory"
typedef unsigned u32x8 __attribute__((ext_vector_type(8)));
DEVI void oproj_a_wg(const Params& p, const Ctl& ctl, int j, int Tq, const Halves hv, LAS char* lds, int w, int lane) {
  const int g = lane >> 4, c = lane & 15, q4 = c >> 2, p4 = c & 3;
  const unsigned lds0 = (unsigned)(size_t)lds;
  const int row = lane >> 2;
  const unsigned vo = row * 1024 + j * 64 + (((lane & 3) ^ (2 * ((row >> 2) & 1))) * 16);
  const unsigned tr0 = lds0 + w * 32768 + (4 * g + q4) * 64 + (((p4 >> 1) ^ (2 * (g & 1))) * 16) + (p4 & 1) * 8;
  const u32x4 ro = mk_rsrc(p.ws + W_O, 32u << 20), rout = mk_rsrc(p.out, SEQ * 2048), rc = mk_rsrc(p.ws + C_CTL, 65536), rw = mk_rsrc(p.awo, 4096 * 1024);
  u32x8 vi = {(unsigned)lane * 16, (unsigned)(CTL_ATA + hv.c + ((Tq * 32 + 8 * w) * 2 + c) * 8) * 4, (unsigned)(lane & 1) + 1, lds0 + w * 2048 + lane * 16,
              lds0 + (w & 1) * 1024 + lane * 16, (unsigned)(c * 2048 + g * 8), tr0, tr0 ^ 32};
  const u32x4 si = {(unsigned)(16 * Tq + 4 * w) * 16384, hv.o, w < 2 ? (unsigned)(16 * Tq * 2048 + (32 * j + 16 * w) * 2) : 0x40000000u, (unsigned)w};
  asm volatile(OPROJ_ASM : "+{v[16:23]}"(vi) : "{v24}"(vo), "{s[36:39]}"(ro), "{s[40:43]}"(rout), "{s[44:47]}"(rc), "{s[48:51]}"(si), "{s[60:63]}"(rw), "{s64}"(ctl.ep) : OPROJ_CLOBBERS);
}

#define ATTB_ASM \
  "v_mov_b32 v182, v188\n\t" \
  "ds_read_b128 v[156:159], v182 offset:0\n\t" \
  "ds_read_b128 v[160:163], v182 offset:1024\n\t" \
  "v_mov_b32 v180, 0xff800000\n\t" \
  "v_mov_b32 v181, 0xff800000\n\t" \
  "s_mov_b32 s40, 31\n\t" \
  "s_waitcnt lgkmcnt(1)\n\t" \
  "v_mfma_f32_32x32x16_bf16 v[76:91], v[156:159], v[192:195], 0\n\t" \
  "v_mfma_f32_32x32x16_bf16 v[92:107], v[156:159], v[196:199], 0\n\t" \
  "s_nop 7\n\t" \
  "Lp1_%=:\n\t" \
  "s_waitcnt lgkmcnt(0)\n\t" \
  "v_mfma_f32_32x32x16_bf16 v[108:123], v[160:163], v[192:195], 0\n\t" \
  "ds_read_b128 v[156:159], v182 offset:2048\n\t" \
  "s_sub_u32 s40, s40, 1\n\t" \
  "v_max3_f32 v180, v180, v76, v77\n\t" \
  "v_max3_f32 v180, v180, v78, v79\n\t" \
  "v_max3_f32 v180, v180, v80, v81\n\t" \
  "v_max3_f32 v180, v180, v82, v83\n\t" \
  "v_max3_f32 v180, v180, v84, v85\n\t" \
  "v_max3_f32 v180, v180, v86, v87\n\t" \
  "v_max3_f32 v180, v180, v88, v89\n\t" \
  "v_max3_f32 v180, v180, v90, v91\n\t" \
  "v_mfma_f32_32x32x16_bf16 v[124:139], v[160:163], v[196:199], 0\n\t" \
  "v_max3_f32 v181, v181, v92, v93\n\t" \
  "v_max3_f32 v181, v181, v94, v95\n\t" \
  "v_max3_f32 v181, v181, v96, v97\n\t" \
  "v_max3_f32 v181, v181, v98, v99\n\t" \
  "v_max3_f32 v181, v181, v100, v101\n\t" \
  "v_max3_f32 v181, v181, v102, v103\n\t" \
  "v_max3_f32 v181, v181, v104, v105\n\t" \
  "v_max3_f32 v181, v181, v106, v107\n\t" \
  "s_waitcnt lgkmcnt(0)\n\t" \
  "v_mfma_f32_32x32x16_bf16 v[76:91], v[156:159], v[192:195], 0\n\t" \
  "ds_read_b128 v[160:163], v182 offset:3072\n\t" \
  "v_add_u32 v182, 2048, v182\n\t" \
  "v_max3_f32 v180, v180, v108, v109\n\t" \
  "v_max3_f32 v180, v180, v110, v111\n\t" \
  "v_max3_f32 v180, v180, v112, v113\n\t" \
  "v_max3_f32 v180, v180, v114, v115\n\t" \
  "v_max3_f32 v180, v180, v116, v117\n\t" \
  "v_max3_f32 v180, v180, v118, v119\n\t" \
  "v_max3_f32 v180, v180, v120, v121\n\t" \
  "v_max3_f32 v180, v180, v122, v123\n\t" \
  "v_mfma_f32_32x32x16_bf16 v[92:107], v[156:159], v[196:199], 0\n\t" \
  "v_max3_f32 v181, v181, v124, v125\n\t" \
  "v_max3_f32 v181, v181, v126, v127\n\t" \
  "v_max3_f32 v181, v181, v128, v129\n\t" \
  "v_max3_f32 v181, v181, v130, v131\n\t" \
  "v_max3_f32 v181, v181, v132, v133\n\t" \
  "v_max3_f32 v181, v181, v134, v135\n\t" \
  "v_max3_f32 v181, v181, v136, v137\n\t" \
  "v_max3_f32 v181, v181, v138, v139\n\t" \
  "s_cbranch_scc0 Lp1_%=\n\t" \
  "v_mov_b32 v185, v180\n\t" \
  "s_nop 1\n\t" \
  "v_permlane32_swap_b32 v180, v185\n\t" \
  "v_max_f32 v180, v180, v185\n\t" \
  "v_mov_b32 v185, v181\n\t" \
  "s_nop 1\n\t" \
  "v_permlane32_swap_b32 v181, v185\n\t" \
  "v_max_f32 v181, v181, v185\n\t" \
  "v_mov_b32 v182, v188\n\t" \
  "v_mov_b32 v183, v189\n\t" \
  "v_mov_b32 v184, v190\n\t" \
  "s_waitcnt lgkmcnt(0)\n\t" \
  "ds_read_b128 v[156:159], v182 offset:0\n\t" \
  "ds_read_b128 v[160:163], v182 offset:1024\n\t" \
  "ds_read_b128 v[164:167], v183 offset:0\n\t" \
  "ds_read_b128 v[168:171], v184 offset:0\n\t" \
  "v_sub_f32 v44, 0, v180\n\t" \
  "v_sub_f32 v45, 0, v180\n\t" \
  "v_sub_f32 v46, 0, v180\n\t" \
  "v_sub_f32 v47, 0, v180\n\t" \
  "v_sub_f32 v48, 0, v180\n\t" \
  "v_sub_f32 v49, 0, v180\n\t" \
  "v_sub_f32 v50, 0, v180\n\t" \
  "v_sub_f32 v51, 0, v180\n\t" \
  "v_sub_f32 v52, 0, v180\n\t" \
  "v_sub_f32 v53, 0, v180\n\t" \
  "v_sub_f32 v54, 0, v180\n\t" \
  "v_sub_f32 v55, 0, v180\n\t" \
  "v_sub_f32 v56, 0, v180\n\t" \
  "v_sub_f32 v57, 0, v180\n\t" \
  "v_sub_f32 v58, 0, v180\n\t" \
  "v_sub_f32 v59, 0, v180\n\t" \
  "v_sub_f32 v60, 0, v181\n\t" \
  "v_sub_f32 v61, 0, v181\n\t" \
  "v_sub_f32 v62, 0, v181\n\t" \
  "v_sub_f32 v63, 0, v181\n\t" \
  "v_sub_f32 v64, 0, v181\n\t" \
  "v_sub_f32 v65, 0, v181\n\t" \
  "v_sub_f32 v66, 0, v181\n\t" \
  "v_sub_f32 v67, 0, v181\n\t" \
  "v_sub_f32 v68, 0, v181\n\t" \
  "v_sub_f32 v69, 0, v181\n\t" \
  "v_sub_f32 v70, 0, v181\n\t" \
  "v_sub_f32 v71, 0, v181\n\t" \
  "v_sub_f32 v72, 0, v181\n\t" \
  "v_sub_f32 v73, 0, v181\n\t" \
  "v_sub_f32 v74, 0, v181\n\t" \
  "v_sub_f32 v75, 0, v181\n\t" \
  "v_mov_b32 v200, 0\n\t" \
  "v_mov_b32 v201, 0\n\t" \
  "v_mov_b32 v202, 0\n\t" \
  "v_mov_b32 v203, 0\n\t" \
  "v_mov_b32 v204, 0\n\t" \
  "v_mov_b32 v205, 0\n\t" \
  "v_mov_b32 v206, 0\n\t" \
  "v_mov_b32 v207, 0\n\t" \
  "v_mov_b32 v208, 0\n\t" \
  "v_mov_b32 v209, 0\n\t" \
  "v_mov_b32 v210, 0\n\t" \
  "v_mov_b32 v211, 0\n\t" \
  "v_mov_b32 v212, 0\n\t" \
  "v_mov_b32 v213, 0\n\t" \
  "v_mov_b32 v214, 0\n\t" \
  "v_mov_b32 v215, 0\n\t" \
  "v_mov_b32 v216, 0\n\t" \
  "v_mov_b32 v217, 0\n\t" \
  "v_mov_b32 v218, 0\n\t" \
  "v_mov_b32 v219, 0\n\t" \
  "v_mov_b32 v220, 0\n\t" \
  "v_mov_b32 v221, 0\n\t" \
  "v_mov_b32 v222, 0\n\t" \
  "v_mov_b32 v223, 0\n\t" \
  "v_mov_b32 v224, 0\n\t" \
  "v_mov_b32 v225, 0\n\t" \
  "v_mov_b32 v226, 0\n\t" \
  "v_mov_b32 v227, 0\n\t" \
  "v_mov_b32 v228, 0\n\t" \
  "v_mov_b32 v229, 0\n\t" \
  "v_mov_b32 v230, 0\n\t" \
  "v_mov_b32 v231, 0\n\t" \
  "v_mov_b32 v148, 0\n\t" \
  "v_mov_b32 v172, 0\n\t" \
  "v_mov_b32 v149, 0\n\t" \
  "v_mov_b32 v173, 0\n\t" \
  "v_mov_b32 v150, 0\n\t" \
  "v_mov_b32 v174, 0\n\t" \
  "v_mov_b32 v151, 0\n\t" \
  "v_mov_b32 v175, 0\n\t" \
  "v_mov_b32 v152, 0\n\t" \
  "v_mov_b32 v176, 0\n\t" \
  "v_mov_b32 v153, 0\n\t" \
  "v_mov_b32 v177, 0\n\t" \
  "v_mov_b32 v154, 0\n\t" \
  "v_mov_b32 v178, 0\n\t" \
  "v_mov_b32 v155, 0\n\t" \
  "v_mov_b32 v179, 0\n\t" \
  "s_mov_b32 s40, 31\n\t" \
  "s_waitcnt lgkmcnt(3)\n\t" \
  "v_mfma_f32_32x32x16_bf16 v[76:91], v[156:159], v[192:195], v[44:59]\n\t" \
  "v_mfma_f32_32x32x16_bf16 v[92:107], v[156:159], v[196:199], v[60:75]\n\t" \
  "s_nop 8\n\t" \
  "Lp2_%=:\n\t" \
  "s_waitcnt lgkmcnt(0)\n\t" \
  "v_mfma_f32_32x32x16_bf16 v[108:123], v[160:163], v[192:195], v[44:59]\n\t" \
  "v_exp_f32 v76, v76\n\t" \
  "ds_read_b128 v[156:159], v182 offset:2048\n\t" \
  "v_exp_f32 v77, v77\n\t" \
  "v_add_u32 v183, v191, v183\n\t" \
  "v_exp_f32 v78, v78\n\t" \
  "v_mfma_f32_32x32x16_bf16 v[216:231], v[172:175], v[148:151], v[216:231]\n\t" \
  "v_exp_f32 v79, v79\n\t" \
  "v_exp_f32 v80, v80\n\t" \
  "v_add_u32 v184, v191, v184\n\t" \
  "v_exp_f32 v81, v81\n\t" \
  "v_cvt_pk_bf16_f32 v140, v76, v77\n\t" \
  "v_exp_f32 v82, v82\n\t" \
  "v_mfma_f32_32x32x16_bf16 v[216:231], v[176:179], v[152:155], v[216:231]\n\t" \
  "v_exp_f32 v83, v83\n\t" \
  "v_cvt_pk_bf16_f32 v141, v78, v79\n\t" \
  "v_exp_f32 v84, v84\n\t" \
  "ds_read_b128 v[172:175], v183 offset:0\n\t" \
  "v_exp_f32 v85, v85\n\t" \
  "v_cvt_pk_bf16_f32 v142, v80, v81\n\t" \
  "v_exp_f32 v86, v86\n\t" \
  "ds_read_b128 v[176:179], v184 offset:0\n\t" \
  "v_exp_f32 v87, v87\n\t" \
  "v_cvt_pk_bf16_f32 v143, v82, v83\n\t" \
  "v_exp_f32 v88, v88\n\t" \
  "s_sub_u32 s40, s40, 1\n\t" \
  "v_exp_f32 v89, v89\n\t" \
  "v_cvt_pk_bf16_f32 v144, v84, v85\n\t" \
  "v_exp_f32 v90, v90\n\t" \
  "v_exp_f32 v91, v91\n\t" \
  "v_cvt_pk_bf16_f32 v145, v86, v87\n\t" \
  "v_cvt_pk_bf16_f32 v146, v88, v89\n\t" \
  "v_cvt_pk_bf16_f32 v147, v90, v91\n\t" \
  "v_mfma_f32_32x32x16_bf16 v[124:139], v[160:163], v[196:199], v[60:75]\n\t" \
  "v_exp_f32 v92, v92\n\t" \
  "v_exp_f32 v93, v93\n\t" \
  "v_exp_f32 v94, v94\n\t" \
  "v_mfma_f32_32x32x16_bf16 v[200:215], v[164:167], v[140:143], v[200:215]\n\t" \
  "v_exp_f32 v95, v95\n\t" \
  "v_exp_f32 v96, v96\n\t" \
  "v_exp_f32 v97, v97\n\t" \
  "v_cvt_pk_bf16_f32 v148, v92, v93\n\t" \
  "v_exp_f32 v98, v98\n\t" \
  "v_mfma_f32_32x32x16_bf16 v[200:215], v[168:171], v[144:147], v[200:215]\n\t" \
  "v_exp_f32 v99, v99\n\t" \
  "v_cvt_pk_bf16_f32 v149, v94, v95\n\t" \
  "v_exp_f32 v100, v100\n\t" \
  "v_exp_f32 v101, v101\n\t" \
  "v_cvt_pk_bf16_f32 v150, v96, v97\n\t" \
  "v_exp_f32 v102, v102\n\t" \
  "v_exp_f32 v103, v103\n\t" \
  "v_cvt_pk_bf16_f32 v151, v98, v99\n\t" \
  "v_exp_f32 v104, v104\n\t" \
  "v_exp_f32 v105, v105\n\t" \
  "v_cvt_pk_bf16_f32 v152, v100, v101\n\t" \
  "v_exp_f32 v106, v106\n\t" \
  "v_exp_f32 v107, v107\n\t" \
  "v_cvt_pk_bf16_f32 v153, v102, v103\n\t" \
  "v_cvt_pk_bf16_f32 v154, v104, v105\n\t" \
  "v_cvt_pk_bf16_f32 v155, v106, v107\n\t" \
  "s_waitcnt lgkmcnt(0)\n\t" \
  "v_mfma_f32_32x32x16_bf16 v[76:91], v[156:159], v[192:195], v[44:59]\n\t" \
  "v_exp_f32 v108, v108\n\t" \
  "ds_read_b128 v[160:163], v182 offset:3072\n\t" \
  "v_exp_f32 v109, v109\n\t" \
  "v_add_u32 v183, v191, v183\n\t" \
  "v_exp_f32 v110, v110\n\t" \
  "v_mfma_f32_32x32x16_bf16 v[216:231], v[164:167], v[148:151], v[216:231]\n\t" \
  "v_exp_f32 v111, v111\n\t" \
  "v_exp_f32 v112, v112\n\t" \
  "v_add_u32 v184, v191, v184\n\t" \
  "v_exp_f32 v113, v113\n\t" \
  "v_cvt_pk_bf16_f32 v140, v108, v109\n\t" \
  "v_exp_f32 v114, v114\n\t" \
  "v_mfma_f32_32x32x16_bf16 v[216:231], v[168:171], v[152:155], v[216:231]\n\t" \
  "v_exp_f32 v115, v115\n\t" \
  "v_cvt_pk_bf16_f32 v141, v110, v111\n\t" \
  "v_exp_f32 v116, v116\n\t" \
  "ds_read_b128 v[164:167], v183 offset:0\n\t" \
  "v_exp_f32 v117, v117\n\t" \
  "v_cvt_pk_bf16_f32 v142, v112, v113\n\t" \
  "v_exp_f32 v118, v118\n\t" \
  "ds_read_b128 v[168:171], v184 offset:0\n\t" \
  "v_exp_f32 v119, v119\n\t" \
  "v_cvt_pk_bf16_f32 v143, v114, v115\n\t" \
  "v_exp_f32 v120, v120\n\t" \
  "v_add_u32 v182, 2048, v182\n\t" \
  "v_exp_f32 v121, v121\n\t" \
  "v_cvt_pk_bf16_f32 v144, v116, v117\n\t" \
  "v_exp_f32 v122, v122\n\t" \
  "v_exp_f32 v123, v123\n\t" \
  "v_cvt_pk_bf16_f32 v145, v118, v119\n\t" \
  "v_cvt_pk_bf16_f32 v146, v120, v121\n\t" \
  "v_cvt_pk_bf16_f32 v147, v122, v123\n\t" \
  "v_mfma_f32_32x32x16_bf16 v[92:107], v[156:159], v[196:199], v[60:75]\n\t" \
  "v_exp_f32 v124, v124\n\t" \
  "v_exp_f32 v125, v125\n\t" \
  "v_exp_f32 v126, v126\n\t" \
  "v_mfma_f32_32x32x16_bf16 v[200:215], v[172:175], v[140:143], v[200:215]\n\t" \
  "v_exp_f32 v127, v127\n\t" \
  "v_exp_f32 v128, v128\n\t" \
  "v_exp_f32 v129, v129\n\t" \
  "v_cvt_pk_bf16_f32 v148, v124, v125\n\t" \
  "v_exp_f32 v130, v130\n\t" \
  "v_mfma_f32_32x32x16_bf16 v[200:215], v[176:179], v[144:147], v[200:215]\n\t" \
  "v_exp_f32 v131, v131\n\t" \
  "v_cvt_pk_bf16_f32 v149, v126, v127\n\t" \
  "v_exp_f32 v132, v132\n\t" \
  "v_exp_f32 v133, v133\n\t" \
  "v_cvt_pk_bf16_f32 v150, v128, v129\n\t" \
  "v_exp_f32 v134, v134\n\t" \
  "v_exp_f32 v135, v135\n\t" \
  "v_cvt_pk_bf16_f32 v151, v130, v131\n\t" \
  "v_exp_f32 v136, v136\n\t" \
  "v_exp_f32 v137, v137\n\t" \
  "v_cvt_pk_bf16_f32 v152, v132, v133\n\t" \
  "v_exp_f32 v138, v138\n\t" \
  "v_exp_f32 v139, v139\n\t" \
  "v_cvt_pk_bf16_f32 v153, v134, v135\n\t" \
  "v_cvt_pk_bf16_f32 v154, v136, v137\n\t" \
  "v_cvt_pk_bf16_f32 v155, v138, v139\n\t" \
  "s_cbranch_scc0 Lp2_%=\n\t" \
  "s_nop 1\n\t" \
  "v_mfma_f32_32x32x16_bf16 v[216:231], v[172:175], v[148:151], v[216:231]\n\t" \
  "v_mfma_f32_32x32x16_bf16 v[216:231], v[176:179], v[152:155], v[216:231]\n\t" \
  "s_waitcnt lgkmcnt(0)\n\t" \
  "s_nop 11\n\t"
#define ATTB_CLOBBERS "v44", "v45", "v46", "v47", "v48", "v49", "v50", "v51", "v52", "v53", "v54", "v55", "v56", "v57", "v58", "v59", "v60", "v61", "v62", "v63", "v64", "v65", "v66", "v67", "v68", "v69", "v70", "v71", "v72", "v73", "v74", "v75", "v76", "v77", "v78", "v79", "v80", "v81", "v82", "v83", "v84", "v85", "v86", "v87", "v88", "v89", "v90", "v91", "v92", "v93", "v94", "v95", "v96", "v97", "v98", "v99", "v100", "v101", "v102", "v103", "v104", "v105", "v106", "v107", "v108", "v109", "v110", "v111", "v112", "v113", "v114", "v115", "v116", "v117", "v118", "v119", "v120", "v121", "v122", "v123", "v124", "v125", "v126", "v127", "v128", "v129", "v130", "v131", "v132", "v133", "v134", "v135", "v136", "v137", "v138", "v139", "v140", "v141", "v142", "v143", "v144", "v145", "v146", "v147", "v148", "v149", "v150", "v151", "v152", "v153", "v154", "v155", "v156", "v157", "v158", "v159", "v160", "v161", "v162", "v163", "v164", "v165", "v166", "v167", "v168", "v169", "v170", "v171", "v172", "v173", "v174", "v175", "v176", "v177", "v178", "v179", "v180", "v181", "v182", "v183", "v184", "v185", "s40", "scc", "memory"
typedef unsigned u32x3 __attribute__((ext_vector_type(3)));
DEVI void attn_b_wg(const Params& p, const Ctl& ctl, int hd, int qfirst, int rounds, int me, LAS char* lds, int w, int lane) {
  const unsigned lds0 = (unsigned)(size_t)lds;
  const rsrc_t rq = mk_rs(p.ws + W_QKVB, 6u << 20), ro = mk_rs(p.ws + W_OB, 2u << 20);
  wait_ge(ctl, CTL_E2 + (((lane >> 3) * 4 + (hd >> 3)) * 2 + ((lane >> 2) & 1)) * 16 + (lane & 3) * 4, lane < 24, 15);
  for (int k = 0; k < 8; ++k) {
    const int so = ((k & 1) + 1) * (2 << 20) + hd * 65536 + (4 * w + (k >> 1)) * 4096;
    #pragma unroll
    for (int n = 0; n < 4; ++n) dma16n<true>(n, rq, lane * 16, so, lds0 + (k & 1) * 65536 + (4 * w + (k >> 1)) * 4096);
  }
  const int r32 = lane & 31, hh = lane >> 5;
  if (w == 0 && lane < 8) *(LAS unsigned*)(lds + 163808 + lane * 4) = lane < 4 ? 0x3f803f80u : 0u;
  const int ka = r32 * 32 + hh * 16;
  const bool vreal = r32 < 16;
  const unsigned va = lds0 + (vreal ? 65536 + (hh * 16 + r32) * 16 : 163808 + (r32 == 16 ? 0 : 16));
  const u32x4 ad = {lds0 + ka, va, va + (vreal ? 512u : 0u), vreal ? 1024u : 0u};
  asm volatile("s_waitcnt vmcnt(0) lgkmcnt(0)\n\ts_barrier" ::: "memory");
  for (int rd = 0; rd < rounds; ++rd) {
    const int q0 = qfirst + rd * 256 + w * 64;
    const u32x4 qa = ld16(rq, ka, hd * 65536 + q0 * 32), qb = ld16(rq, ka, hd * 65536 + (q0 + 32) * 32);
    const u32x8 q = {qa.x, qa.y, qa.z, qa.w, qb.x, qb.y, qb.z, qb.w};
    f32x16 o[2];
    asm volatile(ATTB_ASM : "={v[200:215]}"(o[0]), "={v[216:231]}"(o[1]) : "{v[188:191]}"(ad), "{v[192:199]}"(q) : ATTB_CLOBBERS);
    #pragma unroll
    for (int i = 0; i < 2; ++i) {
      float ot; const float l = swap32(o[i][8], ot);
      const float inv = __builtin_amdgcn_rcpf(l);
      st16((u32x4){pk2(o[i][0] * inv, o[i][1] * inv), pk2(o[i][2] * inv, o[i][3] * inv), pk2(o[i][4] * inv, o[i][5] * inv), pk2(o[i][6] * inv, o[i][7] * inv)},
           ro, r32 * 1024 + hh * 16, (q0 + 32 * i) * 1024 + hd * 32);
    }
  }
  asm volatile("s_waitcnt vmcnt(0)\n\ts_barrier" ::: "memory");
  if (w == 0 && lane == 0) ctl_st(ctl, CTL_ATB + me * 16, 0);
}

DEVI void calibrate(const Params& p, const Ctl& ctl, int page, int slot) {
  const int lane = lane_id();
  char* q = p.ws + (size_t)page * PAGE + 64 * 16384 + slot * 256;
  char* mine = q + (lane & 1) * 8192 + ((lane >> 1) & 3) * 2048;
  unsigned x;
  asm volatile("global_store_dword %1, %2, off sc1\n\ts_waitcnt vmcnt(0)\n\tglobal_load_dword %0, %1, off sc1\n\ts_waitcnt vmcnt(0)\n\t"
               "global_store_dword %1, %2, off sc1\n\ts_waitcnt vmcnt(0)" : "=&v"(x) : "v"(mine), "v"(lane) : "memory");
  unsigned best[2] = {0x7fffu, 0x7fffu};
  #pragma unroll
  for (int r = 0; r < 8; ++r) {
    const unsigned long long t1 = __builtin_amdgcn_s_memtime(); asm volatile("s_waitcnt lgkmcnt(0)" ::: "memory");
    asm volatile("global_load_dword %0, %1, off sc1\n\ts_waitcnt vmcnt(0)" : "=v"(x) : "v"(q + (r & 1) * 8192 + (r >> 1) * 2048) : "memory");
    const unsigned long long t2 = __builtin_amdgcn_s_memtime(); asm volatile("s_waitcnt lgkmcnt(0)" ::: "memory");
    best[r & 1] = min(best[r & 1], (unsigned)(t2 - t1));
  }
  if (lane == 0) ctl_st(ctl, CTL_PG + page * 8 + slot * 2, (int)(best[0] << 16 | best[1]));
}

DEVI void engine_wg(int e, int w, unsigned ep, LAS char* lds) {
  const Params p = args();
  const int lane = lane_id();
  const Ctl ctl{(int*)(p.ws + C_CTL), ep};
  bf16x8 wf[16][4];
  const int x = e >> 5, i = e & 31;
  if (i >= 16) {
    const int u = w * 64 + (i - 16) * 4 + x;
    if (u < 3 * NPAGE) calibrate(p, ctl, u % NPAGE, u / NPAGE);
    for (int k = 0; k < 2; ++k) __builtin_amdgcn_s_sleep(16);
  }
  if (i >= 24) {
    const int r = x * 8 + (i - 24);
    if (r < 24) {
      const int panel = r >> 1;
      load_panel<false>(wf, p.bwqkv, 3072, panel * 128, lds, w, lane);
      const rsrc_t rout = mk_rs(p.ws + W_QKVB, 6u << 20);
      const int so = (panel >> 2) * (2 << 20) + (panel & 3) * 8 * 65536;
      if (panel >= 8) gemm_pairs<EPI_BV>(wf, p.bx, r & 1, 2, 16, rout, so, 1.f, ctl, CTL_E2 + r * 16 + w * 4, lds, w, lane);
      else gemm_pairs<EPI_BQK>(wf, p.bx, r & 1, 2, 16, rout, so, panel < 4 ? 0.25f * 1.4426950408889634f : 1.f, ctl, CTL_E2 + r * 16 + w * 4, lds, w, lane);
    }
    oproj_a_wg(p, ctl, r & 15, r >> 4, wait_halves(ctl, lane), lds, w, lane);
    WG_END();
  }
  if (i >= 16) {
    const int h = x * 8 + (i - 16);
    load_panel<false>(wf, p.awq, 8192, h * 128, lds, w, lane);
    attn_a_wg(p, ctl, wf, h, wait_halves(ctl, lane), lds, w, lane);
    WG_END();
  }
  const int me = x * 16 + i;
  {
    const int m = me & 1, h = me >> 1;
    {
      const rsrc_t rx = mk_rs(p.ax, SEQ * 1024);
      #pragma unroll
      for (int n = 0; n < 4; ++n) dma16n<0>(n, rx, lane * 16, (i * 16 + w * 4) * 1024, (unsigned)(size_t)lds + 131072 + w * 4096);
    }
    load_panel<false>(wf, m == 0 ? p.awk : p.awv, 8192, h * 128, lds, w, lane);
    const int so0 = h;
    const int prog = CTL_E1 + (m * 32 + h) * 16 + w * 4;
    if (m == 1) gemm_pairs<EPI_AV>(wf, p.ax, 0, 1, 32, mk_rs(p.ws + W_VX, 32u << 20), so0, 1.f, ctl, prog, lds, w, lane);
    else gemm_pairs<EPI_AK>(wf, p.ax, 0, 1, 32, mk_rs(p.ws + W_KX, 32u << 20), so0, 1.f, ctl, prog, lds, w, lane);
  }
  attn_b_wg(p, ctl, me >> 1, 1024 + (me & 1) * 512, 2, me, lds, w, lane);
  const int cp = me & 3, rg = me >> 2;
  load_panel<true>(wf, p.bwo, 1024, cp * 128, lds, w, lane);
  wait_ge(ctl, CTL_ATB + (rg < 8 ? 64 + lane : lane * 2 + ((rg >> 2) & 1)) * 16, lane < 32, 0);
  gemm_pairs<EPI_BOUT>(wf, p.ws + W_OB, 2 * rg, 1, 2, mk_rs(p.out, SEQ * 2048), 1024 + cp * 256, 1.f, ctl, 0, lds, w, lane);
  WG_END();
}

DEVI void prefetch_weights(const Params& p, int h, int second, int rot, int w, LAS char* lds) {
  const int lane = lane_id();
  const int vo = (16 * w + (lane >> 4)) * 8192 + h * 256 + (lane & 15) * 16;
  const unsigned ld = (unsigned)(size_t)lds + w * 1024;
  for (int k = 0; k < HDELAY; ++k) __builtin_amdgcn_s_sleep(4);
  for (int k = second; k < 20; k += 2) {
    const rsrc_t r = mk_rs(k < 8 ? p.awq : k < 16 ? p.awk : p.awv, DM * 8192);
    const int so = ((rot + k) & 7) << 19;
    #pragma unroll
    for (int n = 0; n < 4; ++n)
      asm volatile("s_mov_b32 m0, %3\n\ts_nop 0\n\tbuffer_load_dwordx4 %0, %1, %2 offen lds" :: "v"(vo + n * 32768), "s"(r), "s"(so), "s"(ld) : "memory");
    asm volatile("s_waitcnt vmcnt(%0)" :: "i"(HDEPTH * 4) : "memory");
  }
  asm volatile("s_waitcnt vmcnt(0)\n\ts_barrier" ::: "memory");
}

#define CHAIN_ASM \
  "s_mov_b32 s91, 65536\n\t" \
  "s_cmp_eq_u32 s64, 2\n\t" \
  "s_cselect_b32 s65, s71, 0x40000000\n\t" \
  "s_lshr_b32 s93, s75, 1\n\t" \
  "s_add_u32 s95, s93, 145408\n\t" \
  "s_add_u32 s96, s93, 153600\n\t" \
  "v_add_u32 v238, 65536, v232\n\t" \
  "v_add_u32 v239, 65536, v233\n\t" \
  "s_lshr_b32 s94, s75, 1\n\t" \
  "s_mul_i32 s93, s64, 2112\n\t" \
  "s_add_u32 m0, s93, 131072\n\t" \
  "s_nop 0\n\t" \
  "buffer_load_dwordx4 v0, s[28:31], s94 offen lds\n\t" \
  "s_add_u32 m0, s93, 131104\n\t" \
  "s_nop 0\n\t" \
  "buffer_load_dwordx4 v0, s[28:31], s94 offen offset:1024 lds\n\t" \
  "s_mov_b32 s94, 0\n\t" \
  "s_add_u32 m0, s75, 0\n\t" \
  "s_nop 0\n\t" \
  "buffer_load_dwordx4 v228, s[60:63], s94 offen lds\n\t" \
  "s_add_u32 m0, s75, 1024\n\t" \
  "s_nop 0\n\t" \
  "buffer_load_dwordx4 v229, s[60:63], s94 offen lds\n\t" \
  "s_add_u32 m0, s75, 2048\n\t" \
  "s_nop 0\n\t" \
  "buffer_load_dwordx4 v230, s[60:63], s94 offen lds\n\t" \
  "s_add_u32 m0, s75, 3072\n\t" \
  "s_nop 0\n\t" \
  "buffer_load_dwordx4 v231, s[60:63], s94 offen lds\n\t" \
  "s_mov_b32 s94, 256\n\t" \
  "s_add_u32 m0, s75, 16384\n\t" \
  "s_nop 0\n\t" \
  "buffer_load_dwordx4 v228, s[60:63], s94 offen lds\n\t" \
  "s_add_u32 m0, s75, 17408\n\t" \
  "s_nop 0\n\t" \
  "buffer_load_dwordx4 v229, s[60:63], s94 offen lds\n\t" \
  "s_add_u32 m0, s75, 18432\n\t" \
  "s_nop 0\n\t" \
  "buffer_load_dwordx4 v230, s[60:63], s94 offen lds\n\t" \
  "s_add_u32 m0, s75, 19456\n\t" \
  "s_nop 0\n\t" \
  "buffer_load_dwordx4 v231, s[60:63], s94 offen lds\n\t" \
  "s_mov_b32 s94, 65536\n\t" \
  "s_add_u32 m0, s75, 32768\n\t" \
  "s_nop 0\n\t" \
  "buffer_load_dwordx4 v228, s[60:63], s94 offen lds\n\t" \
  "s_add_u32 m0, s75, 33792\n\t" \
  "s_nop 0\n\t" \
  "buffer_load_dwordx4 v229, s[60:63], s94 offen lds\n\t" \
  "s_add_u32 m0, s75, 34816\n\t" \
  "s_nop 0\n\t" \
  "buffer_load_dwordx4 v230, s[60:63], s94 offen lds\n\t" \
  "s_add_u32 m0, s75, 35840\n\t" \
  "s_nop 0\n\t" \
  "buffer_load_dwordx4 v231, s[60:63], s94 offen lds\n\t" \
  "s_mov_b32 s94, 65792\n\t" \
  "s_add_u32 m0, s75, 49152\n\t" \
  "s_nop 0\n\t" \
  "buffer_load_dwordx4 v228, s[60:63], s94 offen lds\n\t" \
  "s_add_u32 m0, s75, 50176\n\t" \
  "s_nop 0\n\t" \
  "buffer_load_dwordx4 v229, s[60:63], s94 offen lds\n\t" \
  "s_add_u32 m0, s75, 51200\n\t" \
  "s_nop 0\n\t" \
  "buffer_load_dwordx4 v230, s[60:63], s94 offen lds\n\t" \
  "s_add_u32 m0, s75, 52224\n\t" \
  "s_nop 0\n\t" \
  "buffer_load_dwordx4 v231, s[60:63], s94 offen lds\n\t" \
  "s_add_u32 s94, s72, 4\n\t" \
  "s_and_b32 s94, s94, 7\n\t" \
  "s_lshl_b32 s94, s94, 19\n\t" \
  "s_add_u32 m0, s75, 65536\n\t" \
  "s_nop 0\n\t" \
  "buffer_load_dwordx4 v224, s[56:59], s94 offen lds\n\t" \
  "s_add_u32 m0, s75, 66560\n\t" \
  "s_nop 0\n\t" \
  "buffer_load_dwordx4 v225, s[56:59], s94 offen lds\n\t" \
  "s_add_u32 m0, s75, 67584\n\t" \
  "s_nop 0\n\t" \
  "buffer_load_dwordx4 v226, s[56:59], s94 offen lds\n\t" \
  "s_add_u32 m0, s75, 68608\n\t" \
  "s_nop 0\n\t" \
  "buffer_load_dwordx4 v227, s[56:59], s94 offen lds\n\t" \
  "s_add_u32 s94, s72, 5\n\t" \
  "s_and_b32 s94, s94, 7\n\t" \
  "s_lshl_b32 s94, s94, 19\n\t" \
  "s_add_u32 m0, s75, 81920\n\t" \
  "s_nop 0\n\t" \
  "buffer_load_dwordx4 v224, s[56:59], s94 offen lds\n\t" \
  "s_add_u32 m0, s75, 82944\n\t" \
  "s_nop 0\n\t" \
  "buffer_load_dwordx4 v225, s[56:59], s94 offen lds\n\t" \
  "s_add_u32 m0, s75, 83968\n\t" \
  "s_nop 0\n\t" \
  "buffer_load_dwordx4 v226, s[56:59], s94 offen lds\n\t" \
  "s_add_u32 m0, s75, 84992\n\t" \
  "s_nop 0\n\t" \
  "buffer_load_dwordx4 v227, s[56:59], s94 offen lds\n\t" \
  "s_add_u32 s94, s72, 6\n\t" \
  "s_and_b32 s94, s94, 7\n\t" \
  "s_lshl_b32 s94, s94, 19\n\t" \
  "s_add_u32 m0, s75, 98304\n\t" \
  "s_nop 0\n\t" \
  "buffer_load_dwordx4 v224, s[56:59], s94 offen lds\n\t" \
  "s_add_u32 m0, s75, 99328\n\t" \
  "s_nop 0\n\t" \
  "buffer_load_dwordx4 v225, s[56:59], s94 offen lds\n\t" \
  "s_add_u32 m0, s75, 100352\n\t" \
  "s_nop 0\n\t" \
  "buffer_load_dwordx4 v226, s[56:59], s94 offen lds\n\t" \
  "s_add_u32 m0, s75, 101376\n\t" \
  "s_nop 0\n\t" \
  "buffer_load_dwordx4 v227, s[56:59], s94 offen lds\n\t" \
  "s_add_u32 s94, s72, 7\n\t" \
  "s_and_b32 s94, s94, 7\n\t" \
  "s_lshl_b32 s94, s94, 19\n\t" \
  "s_add_u32 m0, s75, 114688\n\t" \
  "s_nop 0\n\t" \
  "buffer_load_dwordx4 v224, s[56:59], s94 offen lds\n\t" \
  "s_add_u32 m0, s75, 115712\n\t" \
  "s_nop 0\n\t" \
  "buffer_load_dwordx4 v225, s[56:59], s94 offen lds\n\t" \
  "s_add_u32 m0, s75, 116736\n\t" \
  "s_nop 0\n\t" \
  "buffer_load_dwordx4 v226, s[56:59], s94 offen lds\n\t" \
  "s_add_u32 m0, s75, 117760\n\t" \
  "s_nop 0\n\t" \
  "buffer_load_dwordx4 v227, s[56:59], s94 offen lds\n\t" \
  "s_waitcnt vmcnt(16)\n\t" \
  "s_barrier\n\t" \
  "ds_read_b64_tr_b16 v[48:49], v234 offset:0\n\t" \
  "ds_read_b64_tr_b16 v[50:51], v234 offset:512\n\t" \
  "ds_read_b64_tr_b16 v[52:53], v234 offset:8192\n\t" \
  "ds_read_b64_tr_b16 v[54:55], v234 offset:8704\n\t" \
  "ds_read_b64_tr_b16 v[56:57], v234 offset:32768\n\t" \
  "ds_read_b64_tr_b16 v[58:59], v234 offset:33280\n\t" \
  "ds_read_b64_tr_b16 v[60:61], v234 offset:40960\n\t" \
  "ds_read_b64_tr_b16 v[62:63], v234 offset:41472\n\t" \
  "ds_read_b64_tr_b16 v[192:193], v235 offset:0\n\t" \
  "ds_read_b64_tr_b16 v[194:195], v235 offset:512\n\t" \
  "ds_read_b64_tr_b16 v[196:197], v235 offset:8192\n\t" \
  "ds_read_b64_tr_b16 v[198:199], v235 offset:8704\n\t" \
  "ds_read_b64_tr_b16 v[200:201], v235 offset:32768\n\t" \
  "ds_read_b64_tr_b16 v[202:203], v235 offset:33280\n\t" \
  "ds_read_b64_tr_b16 v[204:205], v235 offset:40960\n\t" \
  "ds_read_b64_tr_b16 v[206:207], v235 offset:41472\n\t" \
  "ds_read_b64_tr_b16 v[208:209], v236 offset:0\n\t" \
  "ds_read_b64_tr_b16 v[210:211], v236 offset:512\n\t" \
  "ds_read_b64_tr_b16 v[212:213], v236 offset:8192\n\t" \
  "ds_read_b64_tr_b16 v[214:215], v236 offset:8704\n\t" \
  "ds_read_b64_tr_b16 v[216:217], v236 offset:32768\n\t" \
  "ds_read_b64_tr_b16 v[218:219], v236 offset:33280\n\t" \
  "ds_read_b64_tr_b16 v[220:221], v236 offset:40960\n\t" \
  "ds_read_b64_tr_b16 v[222:223], v236 offset:41472\n\t" \
  "ds_read_b64_tr_b16 v[240:241], v237 offset:0\n\t" \
  "ds_read_b64_tr_b16 v[242:243], v237 offset:512\n\t" \
  "ds_read_b64_tr_b16 v[244:245], v237 offset:8192\n\t" \
  "ds_read_b64_tr_b16 v[246:247], v237 offset:8704\n\t" \
  "ds_read_b64_tr_b16 v[248:249], v237 offset:32768\n\t" \
  "ds_read_b64_tr_b16 v[250:251], v237 offset:33280\n\t" \
  "ds_read_b64_tr_b16 v[252:253], v237 offset:40960\n\t" \
  "ds_read_b64_tr_b16 v[254:255], v237 offset:41472\n\t" \
  "s_waitcnt lgkmcnt(0)\n\t" \
  "s_barrier\n\t" \
  "s_add_u32 s94, s72, 0\n\t" \
  "s_and_b32 s94, s94, 7\n\t" \
  "s_lshl_b32 s94, s94, 19\n\t" \
  "s_add_u32 m0, s75, 0\n\t" \
  "s_nop 0\n\t" \
  "buffer_load_dwordx4 v224, s[48:51], s94 offen lds\n\t" \
  "s_add_u32 m0, s75, 1024\n\t" \
  "s_nop 0\n\t" \
  "buffer_load_dwordx4 v225, s[48:51], s94 offen lds\n\t" \
  "s_add_u32 m0, s75, 2048\n\t" \
  "s_nop 0\n\t" \
  "buffer_load_dwordx4 v226, s[48:51], s94 offen lds\n\t" \
  "s_add_u32 m0, s75, 3072\n\t" \
  "s_nop 0\n\t" \
  "buffer_load_dwordx4 v227, s[48:51], s94 offen lds\n\t" \
  "s_add_u32 s94, s72, 1\n\t" \
  "s_and_b32 s94, s94, 7\n\t" \
  "s_lshl_b32 s94, s94, 19\n\t" \
  "s_add_u32 m0, s75, 16384\n\t" \
  "s_nop 0\n\t" \
  "buffer_load_dwordx4 v224, s[48:51], s94 offen lds\n\t" \
  "s_add_u32 m0, s75, 17408\n\t" \
  "s_nop 0\n\t" \
  "buffer_load_dwordx4 v225, s[48:51], s94 offen lds\n\t" \
  "s_add_u32 m0, s75, 18432\n\t" \
  "s_nop 0\n\t" \
  "buffer_load_dwordx4 v226, s[48:51], s94 offen lds\n\t" \
  "s_add_u32 m0, s75, 19456\n\t" \
  "s_nop 0\n\t" \
  "buffer_load_dwordx4 v227, s[48:51], s94 offen lds\n\t" \
  "s_add_u32 s94, s72, 2\n\t" \
  "s_and_b32 s94, s94, 7\n\t" \
  "s_lshl_b32 s94, s94, 19\n\t" \
  "s_add_u32 m0, s75, 32768\n\t" \
  "s_nop 0\n\t" \
  "buffer_load_dwordx4 v224, s[48:51], s94 offen lds\n\t" \
  "s_add_u32 m0, s75, 33792\n\t" \
  "s_nop 0\n\t" \
  "buffer_load_dwordx4 v225, s[48:51], s94 offen lds\n\t" \
  "s_add_u32 m0, s75, 34816\n\t" \
  "s_nop 0\n\t" \
  "buffer_load_dwordx4 v226, s[48:51], s94 offen lds\n\t" \
  "s_add_u32 m0, s75, 35840\n\t" \
  "s_nop 0\n\t" \
  "buffer_load_dwordx4 v227, s[48:51], s94 offen lds\n\t" \
  "s_add_u32 s94, s72, 3\n\t" \
  "s_and_b32 s94, s94, 7\n\t" \
  "s_lshl_b32 s94, s94, 19\n\t" \
  "s_add_u32 m0, s75, 49152\n\t" \
  "s_nop 0\n\t" \
  "buffer_load_dwordx4 v224, s[48:51], s94 offen lds\n\t" \
  "s_add_u32 m0, s75, 50176\n\t" \
  "s_nop 0\n\t" \
  "buffer_load_dwordx4 v225, s[48:51], s94 offen lds\n\t" \
  "s_add_u32 m0, s75, 51200\n\t" \
  "s_nop 0\n\t" \
  "buffer_load_dwordx4 v226, s[48:51], s94 offen lds\n\t" \
  "s_add_u32 m0, s75, 52224\n\t" \
  "s_nop 0\n\t" \
  "buffer_load_dwordx4 v227, s[48:51], s94 offen lds\n\t" \
  "s_waitcnt vmcnt(24)\n\t" \
  "s_barrier\n\t" \
  "ds_read_b64_tr_b16 v[128:129], v238 offset:0\n\t" \
  "ds_read_b64_tr_b16 v[130:131], v238 offset:8192\n\t" \
  "ds_read_b64_tr_b16 v[132:133], v238 offset:2048\n\t" \
  "ds_read_b64_tr_b16 v[134:135], v238 offset:10240\n\t" \
  "ds_read_b64_tr_b16 v[136:137], v239 offset:0\n\t" \
  "ds_read_b64_tr_b16 v[138:139], v239 offset:8192\n\t" \
  "ds_read_b64_tr_b16 v[140:141], v239 offset:2048\n\t" \
  "ds_read_b64_tr_b16 v[142:143], v239 offset:10240\n\t" \
  "ds_read_b64_tr_b16 v[144:145], v238 offset:16384\n\t" \
  "ds_read_b64_tr_b16 v[146:147], v238 offset:24576\n\t" \
  "ds_read_b64_tr_b16 v[148:149], v238 offset:18432\n\t" \
  "ds_read_b64_tr_b16 v[150:151], v238 offset:26624\n\t" \
  "ds_read_b64_tr_b16 v[152:153], v239 offset:16384\n\t" \
  "ds_read_b64_tr_b16 v[154:155], v239 offset:24576\n\t" \
  "ds_read_b64_tr_b16 v[156:157], v239 offset:18432\n\t" \
  "ds_read_b64_tr_b16 v[158:159], v239 offset:26624\n\t" \
  "s_waitcnt lgkmcnt(0)\n\t" \
  "s_barrier\n\t" \
  "s_add_u32 s94, s72, 4\n\t" \
  "s_and_b32 s94, s94, 7\n\t" \
  "s_lshl_b32 s94, s94, 19\n\t" \
  "s_add_u32 m0, s75, 65536\n\t" \
  "s_nop 0\n\t" \
  "buffer_load_dwordx4 v224, s[48:51], s94 offen lds\n\t" \
  "s_add_u32 m0, s75, 66560\n\t" \
  "s_nop 0\n\t" \
  "buffer_load_dwordx4 v225, s[48:51], s94 offen lds\n\t" \
  "s_add_u32 m0, s75, 67584\n\t" \
  "s_nop 0\n\t" \
  "buffer_load_dwordx4 v226, s[48:51], s94 offen lds\n\t" \
  "s_add_u32 m0, s75, 68608\n\t" \
  "s_nop 0\n\t" \
  "buffer_load_dwordx4 v227, s[48:51], s94 offen lds\n\t" \
  "s_add_u32 s94, s72, 5\n\t" \
  "s_and_b32 s94, s94, 7\n\t" \
  "s_lshl_b32 s94, s94, 19\n\t" \
  "s_add_u32 m0, s75, 81920\n\t" \
  "s_nop 0\n\t" \
  "buffer_load_dwordx4 v224, s[48:51], s94 offen lds\n\t" \
  "s_add_u32 m0, s75, 82944\n\t" \
  "s_nop 0\n\t" \
  "buffer_load_dwordx4 v225, s[48:51], s94 offen lds\n\t" \
  "s_add_u32 m0, s75, 83968\n\t" \
  "s_nop 0\n\t" \
  "buffer_load_dwordx4 v226, s[48:51], s94 offen lds\n\t" \
  "s_add_u32 m0, s75, 84992\n\t" \
  "s_nop 0\n\t" \
  "buffer_load_dwordx4 v227, s[48:51], s94 offen lds\n\t" \
  "s_waitcnt vmcnt(24)\n\t" \
  "s_barrier\n\t" \
  "ds_read_b64_tr_b16 v[160:161], v238 offset:32768\n\t" \
  "ds_read_b64_tr_b16 v[162:163], v238 offset:40960\n\t" \
  "ds_read_b64_tr_b16 v[164:165], v238 offset:34816\n\t" \
  "ds_read_b64_tr_b16 v[166:167], v238 offset:43008\n\t" \
  "ds_read_b64_tr_b16 v[168:169], v239 offset:32768\n\t" \
  "ds_read_b64_tr_b16 v[170:171], v239 offset:40960\n\t" \
  "ds_read_b64_tr_b16 v[172:173], v239 offset:34816\n\t" \
  "ds_read_b64_tr_b16 v[174:175], v239 offset:43008\n\t" \
  "ds_read_b64_tr_b16 v[176:177], v238 offset:49152\n\t" \
  "ds_read_b64_tr_b16 v[178:179], v238 offset:57344\n\t" \
  "ds_read_b64_tr_b16 v[180:181], v238 offset:51200\n\t" \
  "ds_read_b64_tr_b16 v[182:183], v238 offset:59392\n\t" \
  "ds_read_b64_tr_b16 v[184:185], v239 offset:49152\n\t" \
  "ds_read_b64_tr_b16 v[186:187], v239 offset:57344\n\t" \
  "ds_read_b64_tr_b16 v[188:189], v239 offset:51200\n\t" \
  "ds_read_b64_tr_b16 v[190:191], v239 offset:59392\n\t" \
  "s_waitcnt lgkmcnt(0)\n\t" \
  "s_barrier\n\t" \
  "s_add_u32 s94, s72, 6\n\t" \
  "s_and_b32 s94, s94, 7\n\t" \
  "s_lshl_b32 s94, s94, 19\n\t" \
  "s_add_u32 m0, s75, 98304\n\t" \
  "s_nop 0\n\t" \
  "buffer_load_dwordx4 v224, s[48:51], s94 offen lds\n\t" \
  "s_add_u32 m0, s75, 99328\n\t" \
  "s_nop 0\n\t" \
  "buffer_load_dwordx4 v225, s[48:51], s94 offen lds\n\t" \
  "s_add_u32 m0, s75, 100352\n\t" \
  "s_nop 0\n\t" \
  "buffer_load_dwordx4 v226, s[48:51], s94 offen lds\n\t" \
  "s_add_u32 m0, s75, 101376\n\t" \
  "s_nop 0\n\t" \
  "buffer_load_dwordx4 v227, s[48:51], s94 offen lds\n\t" \
  "s_add_u32 s94, s72, 7\n\t" \
  "s_and_b32 s94, s94, 7\n\t" \
  "s_lshl_b32 s94, s94, 19\n\t" \
  "s_add_u32 m0, s75, 114688\n\t" \
  "s_nop 0\n\t" \
  "buffer_load_dwordx4 v224, s[48:51], s94 offen lds\n\t" \
  "s_add_u32 m0, s75, 115712\n\t" \
  "s_nop 0\n\t" \
  "buffer_load_dwordx4 v225, s[48:51], s94 offen lds\n\t" \
  "s_add_u32 m0, s75, 116736\n\t" \
  "s_nop 0\n\t" \
  "buffer_load_dwordx4 v226, s[48:51], s94 offen lds\n\t" \
  "s_add_u32 m0, s75, 117760\n\t" \
  "s_nop 0\n\t" \
  "buffer_load_dwordx4 v227, s[48:51], s94 offen lds\n\t" \
  "s_waitcnt vmcnt(24)\n\t" \
  "s_barrier\n\t" \
  "ds_read_b64_tr_b16 a[0:1], v232 offset:0\n\t" \
  "ds_read_b64_tr_b16 a[2:3], v232 offset:8192\n\t" \
  "ds_read_b64_tr_b16 a[4:5], v232 offset:2048\n\t" \
  "ds_read_b64_tr_b16 a[6:7], v232 offset:10240\n\t" \
  "ds_read_b64_tr_b16 a[64:65], v233 offset:0\n\t" \
  "ds_read_b64_tr_b16 a[66:67], v233 offset:8192\n\t" \
  "ds_read_b64_tr_b16 a[68:69], v233 offset:2048\n\t" \
  "ds_read_b64_tr_b16 a[70:71], v233 offset:10240\n\t" \
  "ds_read_b64_tr_b16 a[8:9], v232 offset:16384\n\t" \
  "ds_read_b64_tr_b16 a[10:11], v232 offset:24576\n\t" \
  "ds_read_b64_tr_b16 a[12:13], v232 offset:18432\n\t" \
  "ds_read_b64_tr_b16 a[14:15], v232 offset:26624\n\t" \
  "ds_read_b64_tr_b16 a[72:73], v233 offset:16384\n\t" \
  "ds_read_b64_tr_b16 a[74:75], v233 offset:24576\n\t" \
  "ds_read_b64_tr_b16 a[76:77], v233 offset:18432\n\t" \
  "ds_read_b64_tr_b16 a[78:79], v233 offset:26624\n\t" \
  "s_add_u32 s94, s72, 0\n\t" \
  "s_and_b32 s94, s94, 7\n\t" \
  "s_lshl_b32 s94, s94, 7\n\t" \
  "v_add_u32 v231, s94, v40\n\t" \
  "ds_read_b32 v16, v231 offset:0\n\t" \
  "ds_read_b32 v17, v231 offset:32\n\t" \
  "ds_read_b32 v18, v231 offset:64\n\t" \
  "ds_read_b32 v19, v231 offset:96\n\t" \
  "s_add_u32 s94, s72, 1\n\t" \
  "s_and_b32 s94, s94, 7\n\t" \
  "s_lshl_b32 s94, s94, 7\n\t" \
  "v_add_u32 v231, s94, v40\n\t" \
  "ds_read_b32 v20, v231 offset:0\n\t" \
  "ds_read_b32 v21, v231 offset:32\n\t" \
  "ds_read_b32 v22, v231 offset:64\n\t" \
  "ds_read_b32 v23, v231 offset:96\n\t" \
  "s_add_u32 s94, s72, 2\n\t" \
  "s_and_b32 s94, s94, 7\n\t" \
  "s_lshl_b32 s94, s94, 7\n\t" \
  "v_add_u32 v231, s94, v40\n\t" \
  "ds_read_b32 v24, v231 offset:0\n\t" \
  "ds_read_b32 v25, v231 offset:32\n\t" \
  "ds_read_b32 v26, v231 offset:64\n\t" \
  "ds_read_b32 v27, v231 offset:96\n\t" \
  "s_add_u32 s94, s72, 3\n\t" \
  "s_and_b32 s94, s94, 7\n\t" \
  "s_lshl_b32 s94, s94, 7\n\t" \
  "v_add_u32 v231, s94, v40\n\t" \
  "ds_read_b32 v30, v231 offset:0\n\t" \
  "ds_read_b32 v31, v231 offset:32\n\t" \
  "ds_read_b32 v32, v231 offset:64\n\t" \
  "ds_read_b32 v33, v231 offset:96\n\t" \
  "s_add_u32 s94, s72, 4\n\t" \
  "s_and_b32 s94, s94, 7\n\t" \
  "s_lshl_b32 s94, s94, 7\n\t" \
  "v_add_u32 v231, s94, v40\n\t" \
  "ds_read_b32 v34, v231 offset:0\n\t" \
  "ds_read_b32 v35, v231 offset:32\n\t" \
  "ds_read_b32 v36, v231 offset:64\n\t" \
  "ds_read_b32 v37, v231 offset:96\n\t" \
  "s_add_u32 s94, s72, 5\n\t" \
  "s_and_b32 s94, s94, 7\n\t" \
  "s_lshl_b32 s94, s94, 7\n\t" \
  "v_add_u32 v231, s94, v40\n\t" \
  "ds_read_b32 v38, v231 offset:0\n\t" \
  "ds_read_b32 v39, v231 offset:32\n\t" \
  "ds_read_b32 v41, v231 offset:64\n\t" \
  "ds_read_b32 v42, v231 offset:96\n\t" \
  "s_add_u32 s94, s72, 6\n\t" \
  "s_and_b32 s94, s94, 7\n\t" \
  "s_lshl_b32 s94, s94, 7\n\t" \
  "v_add_u32 v231, s94, v40\n\t" \
  "ds_read_b32 v43, v231 offset:0\n\t" \
  "ds_read_b32 v44, v231 offset:32\n\t" \
  "ds_read_b32 v45, v231 offset:64\n\t" \
  "ds_read_b32 v46, v231 offset:96\n\t" \
  "s_add_u32 s94, s72, 7\n\t" \
  "s_and_b32 s94, s94, 7\n\t" \
  "s_lshl_b32 s94, s94, 7\n\t" \
  "v_add_u32 v231, s94, v40\n\t" \
  "ds_read_b32 v47, v231 offset:0\n\t" \
  "ds_read_b32 v228, v231 offset:32\n\t" \
  "ds_read_b32 v229, v231 offset:64\n\t" \
  "ds_read_b32 v230, v231 offset:96\n\t" \
  "s_waitcnt lgkmcnt(0)\n\t" \
  "s_barrier\n\t" \
  "s_add_u32 s94, s72, 0\n\t" \
  "s_and_b32 s94, s94, 7\n\t" \
  "s_lshl_b32 s94, s94, 19\n\t" \
  "s_add_u32 m0, s75, 0\n\t" \
  "s_nop 0\n\t" \
  "buffer_load_dwordx4 v224, s[52:55], s94 offen lds\n\t" \
  "s_add_u32 m0, s75, 1024\n\t" \
  "s_nop 0\n\t" \
  "buffer_load_dwordx4 v225, s[52:55], s94 offen lds\n\t" \
  "s_add_u32 m0, s75, 2048\n\t" \
  "s_nop 0\n\t" \
  "buffer_load_dwordx4 v226, s[52:55], s94 offen lds\n\t" \
  "s_add_u32 m0, s75, 3072\n\t" \
  "s_nop 0\n\t" \
  "buffer_load_dwordx4 v227, s[52:55], s94 offen lds\n\t" \
  "s_add_u32 s94, s72, 1\n\t" \
  "s_and_b32 s94, s94, 7\n\t" \
  "s_lshl_b32 s94, s94, 19\n\t" \
  "s_add_u32 m0, s75, 16384\n\t" \
  "s_nop 0\n\t" \
  "buffer_load_dwordx4 v224, s[52:55], s94 offen lds\n\t" \
  "s_add_u32 m0, s75, 17408\n\t" \
  "s_nop 0\n\t" \
  "buffer_load_dwordx4 v225, s[52:55], s94 offen lds\n\t" \
  "s_add_u32 m0, s75, 18432\n\t" \
  "s_nop 0\n\t" \
  "buffer_load_dwordx4 v226, s[52:55], s94 offen lds\n\t" \
  "s_add_u32 m0, s75, 19456\n\t" \
  "s_nop 0\n\t" \
  "buffer_load_dwordx4 v227, s[52:55], s94 offen lds\n\t" \
  "s_waitcnt vmcnt(24)\n\t" \
  "s_barrier\n\t" \
  "ds_read_b64_tr_b16 a[16:17], v232 offset:32768\n\t" \
  "ds_read_b64_tr_b16 a[18:19], v232 offset:40960\n\t" \
  "ds_read_b64_tr_b16 a[20:21], v232 offset:34816\n\t" \
  "ds_read_b64_tr_b16 a[22:23], v232 offset:43008\n\t" \
  "ds_read_b64_tr_b16 a[80:81], v233 offset:32768\n\t" \
  "ds_read_b64_tr_b16 a[82:83], v233 offset:40960\n\t" \
  "ds_read_b64_tr_b16 a[84:85], v233 offset:34816\n\t" \
  "ds_read_b64_tr_b16 a[86:87], v233 offset:43008\n\t" \
  "ds_read_b64_tr_b16 a[24:25], v232 offset:49152\n\t" \
  "ds_read_b64_tr_b16 a[26:27], v232 offset:57344\n\t" \
  "ds_read_b64_tr_b16 a[28:29], v232 offset:51200\n\t" \
  "ds_read_b64_tr_b16 a[30:31], v232 offset:59392\n\t" \
  "ds_read_b64_tr_b16 a[88:89], v233 offset:49152\n\t" \
  "ds_read_b64_tr_b16 a[90:91], v233 offset:57344\n\t" \
  "ds_read_b64_tr_b16 a[92:93], v233 offset:51200\n\t" \
  "ds_read_b64_tr_b16 a[94:95], v233 offset:59392\n\t" \
  "s_waitcnt lgkmcnt(0)\n\t" \
  "s_barrier\n\t" \
  "s_add_u32 s94, s72, 2\n\t" \
  "s_and_b32 s94, s94, 7\n\t" \
  "s_lshl_b32 s94, s94, 19\n\t" \
  "s_add_u32 m0, s75, 32768\n\t" \
  "s_nop 0\n\t" \
  "buffer_load_dwordx4 v224, s[52:55], s94 offen lds\n\t" \
  "s_add_u32 m0, s75, 33792\n\t" \
  "s_nop 0\n\t" \
  "buffer_load_dwordx4 v225, s[52:55], s94 offen lds\n\t" \
  "s_add_u32 m0, s75, 34816\n\t" \
  "s_nop 0\n\t" \
  "buffer_load_dwordx4 v226, s[52:55], s94 offen lds\n\t" \
  "s_add_u32 m0, s75, 35840\n\t" \
  "s_nop 0\n\t" \
  "buffer_load_dwordx4 v227, s[52:55], s94 offen lds\n\t" \
  "s_add_u32 s94, s72, 3\n\t" \
  "s_and_b32 s94, s94, 7\n\t" \
  "s_lshl_b32 s94, s94, 19\n\t" \
  "s_add_u32 m0, s75, 49152\n\t" \
  "s_nop 0\n\t" \
  "buffer_load_dwordx4 v224, s[52:55], s94 offen lds\n\t" \
  "s_add_u32 m0, s75, 50176\n\t" \
  "s_nop 0\n\t" \
  "buffer_load_dwordx4 v225, s[52:55], s94 offen lds\n\t" \
  "s_add_u32 m0, s75, 51200\n\t" \
  "s_nop 0\n\t" \
  "buffer_load_dwordx4 v226, s[52:55], s94 offen lds\n\t" \
  "s_add_u32 m0, s75, 52224\n\t" \
  "s_nop 0\n\t" \
  "buffer_load_dwordx4 v227, s[52:55], s94 offen lds\n\t" \
  "s_waitcnt vmcnt(24)\n\t" \
  "s_barrier\n\t" \
  "ds_read_b64_tr_b16 a[32:33], v238 offset:0\n\t" \
  "ds_read_b64_tr_b16 a[34:35], v238 offset:8192\n\t" \
  "ds_read_b64_tr_b16 a[36:37], v238 offset:2048\n\t" \
  "ds_read_b64_tr_b16 a[38:39], v238 offset:10240\n\t" \
  "ds_read_b64_tr_b16 a[96:97], v239 offset:0\n\t" \
  "ds_read_b64_tr_b16 a[98:99], v239 offset:8192\n\t" \
  "ds_read_b64_tr_b16 a[100:101], v239 offset:2048\n\t" \
  "ds_read_b64_tr_b16 a[102:103], v239 offset:10240\n\t" \
  "ds_read_b64_tr_b16 a[40:41], v238 offset:16384\n\t" \
  "ds_read_b64_tr_b16 a[42:43], v238 offset:24576\n\t" \
  "ds_read_b64_tr_b16 a[44:45], v238 offset:18432\n\t" \
  "ds_read_b64_tr_b16 a[46:47], v238 offset:26624\n\t" \
  "ds_read_b64_tr_b16 a[104:105], v239 offset:16384\n\t" \
  "ds_read_b64_tr_b16 a[106:107], v239 offset:24576\n\t" \
  "ds_read_b64_tr_b16 a[108:109], v239 offset:18432\n\t" \
  "ds_read_b64_tr_b16 a[110:111], v239 offset:26624\n\t" \
  "s_waitcnt lgkmcnt(0)\n\t" \
  "s_barrier\n\t" \
  "s_add_u32 s94, s72, 4\n\t" \
  "s_and_b32 s94, s94, 7\n\t" \
  "s_lshl_b32 s94, s94, 19\n\t" \
  "s_add_u32 m0, s75, 65536\n\t" \
  "s_nop 0\n\t" \
  "buffer_load_dwordx4 v224, s[52:55], s94 offen lds\n\t" \
  "s_add_u32 m0, s75, 66560\n\t" \
  "s_nop 0\n\t" \
  "buffer_load_dwordx4 v225, s[52:55], s94 offen lds\n\t" \
  "s_add_u32 m0, s75, 67584\n\t" \
  "s_nop 0\n\t" \
  "buffer_load_dwordx4 v226, s[52:55], s94 offen lds\n\t" \
  "s_add_u32 m0, s75, 68608\n\t" \
  "s_nop 0\n\t" \
  "buffer_load_dwordx4 v227, s[52:55], s94 offen lds\n\t" \
  "s_add_u32 s94, s72, 5\n\t" \
  "s_and_b32 s94, s94, 7\n\t" \
  "s_lshl_b32 s94, s94, 19\n\t" \
  "s_add_u32 m0, s75, 81920\n\t" \
  "s_nop 0\n\t" \
  "buffer_load_dwordx4 v224, s[52:55], s94 offen lds\n\t" \
  "s_add_u32 m0, s75, 82944\n\t" \
  "s_nop 0\n\t" \
  "buffer_load_dwordx4 v225, s[52:55], s94 offen lds\n\t" \
  "s_add_u32 m0, s75, 83968\n\t" \
  "s_nop 0\n\t" \
  "buffer_load_dwordx4 v226, s[52:55], s94 offen lds\n\t" \
  "s_add_u32 m0, s75, 84992\n\t" \
  "s_nop 0\n\t" \
  "buffer_load_dwordx4 v227, s[52:55], s94 offen lds\n\t" \
  "s_waitcnt vmcnt(24)\n\t" \
  "s_barrier\n\t" \
  "ds_read_b64_tr_b16 a[48:49], v238 offset:32768\n\t" \
  "ds_read_b64_tr_b16 a[50:51], v238 offset:40960\n\t" \
  "ds_read_b64_tr_b16 a[52:53], v238 offset:34816\n\t" \
  "ds_read_b64_tr_b16 a[54:55], v238 offset:43008\n\t" \
  "ds_read_b64_tr_b16 a[112:113], v239 offset:32768\n\t" \
  "ds_read_b64_tr_b16 a[114:115], v239 offset:40960\n\t" \
  "ds_read_b64_tr_b16 a[116:117], v239 offset:34816\n\t" \
  "ds_read_b64_tr_b16 a[118:119], v239 offset:43008\n\t" \
  "ds_read_b64_tr_b16 a[56:57], v238 offset:49152\n\t" \
  "ds_read_b64_tr_b16 a[58:59], v238 offset:57344\n\t" \
  "ds_read_b64_tr_b16 a[60:61], v238 offset:51200\n\t" \
  "ds_read_b64_tr_b16 a[62:63], v238 offset:59392\n\t" \
  "ds_read_b64_tr_b16 a[120:121], v239 offset:49152\n\t" \
  "ds_read_b64_tr_b16 a[122:123], v239 offset:57344\n\t" \
  "ds_read_b64_tr_b16 a[124:125], v239 offset:51200\n\t" \
  "ds_read_b64_tr_b16 a[126:127], v239 offset:59392\n\t" \
  "s_waitcnt lgkmcnt(0)\n\t" \
  "s_barrier\n\t" \
  "s_add_u32 s94, s72, 6\n\t" \
  "s_and_b32 s94, s94, 7\n\t" \
  "s_lshl_b32 s94, s94, 19\n\t" \
  "s_add_u32 m0, s75, 98304\n\t" \
  "s_nop 0\n\t" \
  "buffer_load_dwordx4 v224, s[52:55], s94 offen lds\n\t" \
  "s_add_u32 m0, s75, 99328\n\t" \
  "s_nop 0\n\t" \
  "buffer_load_dwordx4 v225, s[52:55], s94 offen lds\n\t" \
  "s_add_u32 m0, s75, 100352\n\t" \
  "s_nop 0\n\t" \
  "buffer_load_dwordx4 v226, s[52:55], s94 offen lds\n\t" \
  "s_add_u32 m0, s75, 101376\n\t" \
  "s_nop 0\n\t" \
  "buffer_load_dwordx4 v227, s[52:55], s94 offen lds\n\t" \
  "s_add_u32 s94, s72, 7\n\t" \
  "s_and_b32 s94, s94, 7\n\t" \
  "s_lshl_b32 s94, s94, 19\n\t" \
  "s_add_u32 m0, s75, 114688\n\t" \
  "s_nop 0\n\t" \
  "buffer_load_dwordx4 v224, s[52:55], s94 offen lds\n\t" \
  "s_add_u32 m0, s75, 115712\n\t" \
  "s_nop 0\n\t" \
  "buffer_load_dwordx4 v225, s[52:55], s94 offen lds\n\t" \
  "s_add_u32 m0, s75, 116736\n\t" \
  "s_nop 0\n\t" \
  "buffer_load_dwordx4 v226, s[52:55], s94 offen lds\n\t" \
  "s_add_u32 m0, s75, 117760\n\t" \
  "s_nop 0\n\t" \
  "buffer_load_dwordx4 v227, s[52:55], s94 offen lds\n\t" \
  "s_waitcnt vmcnt(24)\n\t" \
  "s_barrier\n\t" \
  "ds_read_b64_tr_b16 a[128:129], v232 offset:0\n\t" \
  "ds_read_b64_tr_b16 a[130:131], v232 offset:8192\n\t" \
  "ds_read_b64_tr_b16 a[132:133], v232 offset:2048\n\t" \
  "ds_read_b64_tr_b16 a[134:135], v232 offset:10240\n\t" \
  "ds_read_b64_tr_b16 a[192:193], v233 offset:0\n\t" \
  "ds_read_b64_tr_b16 a[194:195], v233 offset:8192\n\t" \
  "ds_read_b64_tr_b16 a[196:197], v233 offset:2048\n\t" \
  "ds_read_b64_tr_b16 a[198:199], v233 offset:10240\n\t" \
  "ds_read_b64_tr_b16 a[136:137], v232 offset:16384\n\t" \
  "ds_read_b64_tr_b16 a[138:139], v232 offset:24576\n\t" \
  "ds_read_b64_tr_b16 a[140:141], v232 offset:18432\n\t" \
  "ds_read_b64_tr_b16 a[142:143], v232 offset:26624\n\t" \
  "ds_read_b64_tr_b16 a[200:201], v233 offset:16384\n\t" \
  "ds_read_b64_tr_b16 a[202:203], v233 offset:24576\n\t" \
  "ds_read_b64_tr_b16 a[204:205], v233 offset:18432\n\t" \
  "ds_read_b64_tr_b16 a[206:207], v233 offset:26624\n\t" \
  "s_waitcnt lgkmcnt(0)\n\t" \
  "s_barrier\n\t" \
  "s_add_u32 s94, s72, 0\n\t" \
  "s_and_b32 s94, s94, 7\n\t" \
  "s_lshl_b32 s94, s94, 19\n\t" \
  "s_add_u32 m0, s75, 0\n\t" \
  "s_nop 0\n\t" \
  "buffer_load_dwordx4 v224, s[56:59], s94 offen lds\n\t" \
  "s_add_u32 m0, s75, 1024\n\t" \
  "s_nop 0\n\t" \
  "buffer_load_dwordx4 v225, s[56:59], s94 offen lds\n\t" \
  "s_add_u32 m0, s75, 2048\n\t" \
  "s_nop 0\n\t" \
  "buffer_load_dwordx4 v226, s[56:59], s94 offen lds\n\t" \
  "s_add_u32 m0, s75, 3072\n\t" \
  "s_nop 0\n\t" \
  "buffer_load_dwordx4 v227, s[56:59], s94 offen lds\n\t" \
  "s_add_u32 s94, s72, 1\n\t" \
  "s_and_b32 s94, s94, 7\n\t" \
  "s_lshl_b32 s94, s94, 19\n\t" \
  "s_add_u32 m0, s75, 16384\n\t" \
  "s_nop 0\n\t" \
  "buffer_load_dwordx4 v224, s[56:59], s94 offen lds\n\t" \
  "s_add_u32 m0, s75, 17408\n\t" \
  "s_nop 0\n\t" \
  "buffer_load_dwordx4 v225, s[56:59], s94 offen lds\n\t" \
  "s_add_u32 m0, s75, 18432\n\t" \
  "s_nop 0\n\t" \
  "buffer_load_dwordx4 v226, s[56:59], s94 offen lds\n\t" \
  "s_add_u32 m0, s75, 19456\n\t" \
  "s_nop 0\n\t" \
  "buffer_load_dwordx4 v227, s[56:59], s94 offen lds\n\t" \
  "s_waitcnt vmcnt(24)\n\t" \
  "s_barrier\n\t" \
  "ds_read_b64_tr_b16 a[144:145], v232 offset:32768\n\t" \
  "ds_read_b64_tr_b16 a[146:147], v232 offset:40960\n\t" \
  "ds_read_b64_tr_b16 a[148:149], v232 offset:34816\n\t" \
  "ds_read_b64_tr_b16 a[150:151], v232 offset:43008\n\t" \
  "ds_read_b64_tr_b16 a[208:209], v233 offset:32768\n\t" \
  "ds_read_b64_tr_b16 a[210:211], v233 offset:40960\n\t" \
  "ds_read_b64_tr_b16 a[212:213], v233 offset:34816\n\t" \
  "ds_read_b64_tr_b16 a[214:215], v233 offset:43008\n\t" \
  "ds_read_b64_tr_b16 a[152:153], v232 offset:49152\n\t" \
  "ds_read_b64_tr_b16 a[154:155], v232 offset:57344\n\t" \
  "ds_read_b64_tr_b16 a[156:157], v232 offset:51200\n\t" \
  "ds_read_b64_tr_b16 a[158:159], v232 offset:59392\n\t" \
  "ds_read_b64_tr_b16 a[216:217], v233 offset:49152\n\t" \
  "ds_read_b64_tr_b16 a[218:219], v233 offset:57344\n\t" \
  "ds_read_b64_tr_b16 a[220:221], v233 offset:51200\n\t" \
  "ds_read_b64_tr_b16 a[222:223], v233 offset:59392\n\t" \
  "s_waitcnt lgkmcnt(0)\n\t" \
  "s_barrier\n\t" \
  "s_add_u32 s94, s72, 2\n\t" \
  "s_and_b32 s94, s94, 7\n\t" \
  "s_lshl_b32 s94, s94, 19\n\t" \
  "s_add_u32 m0, s75, 32768\n\t" \
  "s_nop 0\n\t" \
  "buffer_load_dwordx4 v224, s[56:59], s94 offen lds\n\t" \
  "s_add_u32 m0, s75, 33792\n\t" \
  "s_nop 0\n\t" \
  "buffer_load_dwordx4 v225, s[56:59], s94 offen lds\n\t" \
  "s_add_u32 m0, s75, 34816\n\t" \
  "s_nop 0\n\t" \
  "buffer_load_dwordx4 v226, s[56:59], s94 offen lds\n\t" \
  "s_add_u32 m0, s75, 35840\n\t" \
  "s_nop 0\n\t" \
  "buffer_load_dwordx4 v227, s[56:59], s94 offen lds\n\t" \
  "s_add_u32 s94, s72, 3\n\t" \
  "s_and_b32 s94, s94, 7\n\t" \
  "s_lshl_b32 s94, s94, 19\n\t" \
  "s_add_u32 m0, s75, 49152\n\t" \
  "s_nop 0\n\t" \
  "buffer_load_dwordx4 v224, s[56:59], s94 offen lds\n\t" \
  "s_add_u32 m0, s75, 50176\n\t" \
  "s_nop 0\n\t" \
  "buffer_load_dwordx4 v225, s[56:59], s94 offen lds\n\t" \
  "s_add_u32 m0, s75, 51200\n\t" \
  "s_nop 0\n\t" \
  "buffer_load_dwordx4 v226, s[56:59], s94 offen lds\n\t" \
  "s_add_u32 m0, s75, 52224\n\t" \
  "s_nop 0\n\t" \
  "buffer_load_dwordx4 v227, s[56:59], s94 offen lds\n\t" \
  "s_waitcnt vmcnt(24)\n\t" \
  "s_barrier\n\t" \
  "ds_read_b64_tr_b16 a[160:161], v238 offset:0\n\t" \
  "ds_read_b64_tr_b16 a[162:163], v238 offset:8192\n\t" \
  "ds_read_b64_tr_b16 a[164:165], v238 offset:2048\n\t" \
  "ds_read_b64_tr_b16 a[166:167], v238 offset:10240\n\t" \
  "ds_read_b64_tr_b16 a[224:225], v239 offset:0\n\t" \
  "ds_read_b64_tr_b16 a[226:227], v239 offset:8192\n\t" \
  "ds_read_b64_tr_b16 a[228:229], v239 offset:2048\n\t" \
  "ds_read_b64_tr_b16 a[230:231], v239 offset:10240\n\t" \
  "ds_read_b64_tr_b16 a[168:169], v238 offset:16384\n\t" \
  "ds_read_b64_tr_b16 a[170:171], v238 offset:24576\n\t" \
  "ds_read_b64_tr_b16 a[172:173], v238 offset:18432\n\t" \
  "ds_read_b64_tr_b16 a[174:175], v238 offset:26624\n\t" \
  "ds_read_b64_tr_b16 a[232:233], v239 offset:16384\n\t" \
  "ds_read_b64_tr_b16 a[234:235], v239 offset:24576\n\t" \
  "ds_read_b64_tr_b16 a[236:237], v239 offset:18432\n\t" \
  "ds_read_b64_tr_b16 a[238:239], v239 offset:26624\n\t" \
  "s_waitcnt lgkmcnt(0)\n\t" \
  "s_barrier\n\t" \
  "s_waitcnt vmcnt(16)\n\t" \
  "s_barrier\n\t" \
  "ds_read_b64_tr_b16 a[176:177], v238 offset:32768\n\t" \
  "ds_read_b64_tr_b16 a[178:179], v238 offset:40960\n\t" \
  "ds_read_b64_tr_b16 a[180:181], v238 offset:34816\n\t" \
  "ds_read_b64_tr_b16 a[182:183], v238 offset:43008\n\t" \
  "ds_read_b64_tr_b16 a[240:241], v239 offset:32768\n\t" \
  "ds_read_b64_tr_b16 a[242:243], v239 offset:40960\n\t" \
  "ds_read_b64_tr_b16 a[244:245], v239 offset:34816\n\t" \
  "ds_read_b64_tr_b16 a[246:247], v239 offset:43008\n\t" \
  "ds_read_b64_tr_b16 a[184:185], v238 offset:49152\n\t" \
  "ds_read_b64_tr_b16 a[186:187], v238 offset:57344\n\t" \
  "ds_read_b64_tr_b16 a[188:189], v238 offset:51200\n\t" \
  "ds_read_b64_tr_b16 a[190:191], v238 offset:59392\n\t" \
  "ds_read_b64_tr_b16 a[248:249], v239 offset:49152\n\t" \
  "ds_read_b64_tr_b16 a[250:251], v239 offset:57344\n\t" \
  "ds_read_b64_tr_b16 a[252:253], v239 offset:51200\n\t" \
  "ds_read_b64_tr_b16 a[254:255], v239 offset:59392\n\t" \
  "s_waitcnt lgkmcnt(0)\n\t" \
  "s_barrier\n\t" \
  "s_waitcnt vmcnt(8)\n\t" \
  "s_barrier\n\t" \
  "ds_read_b64_tr_b16 v[64:65], v232 offset:0\n\t" \
  "ds_read_b64_tr_b16 v[66:67], v232 offset:8192\n\t" \
  "ds_read_b64_tr_b16 v[68:69], v232 offset:2048\n\t" \
  "ds_read_b64_tr_b16 v[70:71], v232 offset:10240\n\t" \
  "ds_read_b64_tr_b16 v[72:73], v233 offset:0\n\t" \
  "ds_read_b64_tr_b16 v[74:75], v233 offset:8192\n\t" \
  "ds_read_b64_tr_b16 v[76:77], v233 offset:2048\n\t" \
  "ds_read_b64_tr_b16 v[78:79], v233 offset:10240\n\t" \
  "ds_read_b64_tr_b16 v[80:81], v232 offset:16384\n\t" \
  "ds_read_b64_tr_b16 v[82:83], v232 offset:24576\n\t" \
  "ds_read_b64_tr_b16 v[84:85], v232 offset:18432\n\t" \
  "ds_read_b64_tr_b16 v[86:87], v232 offset:26624\n\t" \
  "ds_read_b64_tr_b16 v[88:89], v233 offset:16384\n\t" \
  "ds_read_b64_tr_b16 v[90:91], v233 offset:24576\n\t" \
  "ds_read_b64_tr_b16 v[92:93], v233 offset:18432\n\t" \
  "ds_read_b64_tr_b16 v[94:95], v233 offset:26624\n\t" \
  "s_waitcnt lgkmcnt(0)\n\t" \
  "s_barrier\n\t" \
  "s_waitcnt vmcnt(0)\n\t" \
  "s_barrier\n\t" \
  "ds_read_b64_tr_b16 v[96:97], v232 offset:32768\n\t" \
  "ds_read_b64_tr_b16 v[98:99], v232 offset:40960\n\t" \
  "ds_read_b64_tr_b16 v[100:101], v232 offset:34816\n\t" \
  "ds_read_b64_tr_b16 v[102:103], v232 offset:43008\n\t" \
  "ds_read_b64_tr_b16 v[104:105], v233 offset:32768\n\t" \
  "ds_read_b64_tr_b16 v[106:107], v233 offset:40960\n\t" \
  "ds_read_b64_tr_b16 v[108:109], v233 offset:34816\n\t" \
  "ds_read_b64_tr_b16 v[110:111], v233 offset:43008\n\t" \
  "ds_read_b64_tr_b16 v[112:113], v232 offset:49152\n\t" \
  "ds_read_b64_tr_b16 v[114:115], v232 offset:57344\n\t" \
  "ds_read_b64_tr_b16 v[116:117], v232 offset:51200\n\t" \
  "ds_read_b64_tr_b16 v[118:119], v232 offset:59392\n\t" \
  "ds_read_b64_tr_b16 v[120:121], v233 offset:49152\n\t" \
  "ds_read_b64_tr_b16 v[122:123], v233 offset:57344\n\t" \
  "ds_read_b64_tr_b16 v[124:125], v233 offset:51200\n\t" \
  "ds_read_b64_tr_b16 v[126:127], v233 offset:59392\n\t" \
  "s_waitcnt lgkmcnt(0)\n\t" \
  "s_barrier\n\t" \
  "ds_write_b128 v9, v[128:131] offset:0\n\t" \
  "ds_write_b128 v9, v[132:135] offset:1024\n\t" \
  "ds_write_b128 v8, v[48:51] offset:0\n\t" \
  "ds_write_b128 v8, v[52:55] offset:1024\n\t" \
  "ds_write_b128 v9, v[136:139] offset:2048\n\t" \
  "ds_write_b128 v9, v[140:143] offset:3072\n\t" \
  "ds_write_b128 v8, v[56:59] offset:2048\n\t" \
  "ds_write_b128 v8, v[60:63] offset:3072\n\t" \
  "ds_write_b128 v9, v[144:147] offset:4096\n\t" \
  "ds_write_b128 v9, v[148:151] offset:5120\n\t" \
  "ds_write_b128 v8, v[192:195] offset:4096\n\t" \
  "ds_write_b128 v8, v[196:199] offset:5120\n\t" \
  "ds_write_b128 v9, v[152:155] offset:6144\n\t" \
  "ds_write_b128 v9, v[156:159] offset:7168\n\t" \
  "ds_write_b128 v8, v[200:203] offset:6144\n\t" \
  "ds_write_b128 v8, v[204:207] offset:7168\n\t" \
  "ds_write_b128 v9, v[160:163] offset:8192\n\t" \
  "ds_write_b128 v9, v[164:167] offset:9216\n\t" \
  "ds_write_b128 v8, v[208:211] offset:8192\n\t" \
  "ds_write_b128 v8, v[212:215] offset:9216\n\t" \
  "ds_write_b128 v9, v[168:171] offset:10240\n\t" \
  "ds_write_b128 v9, v[172:175] offset:11264\n\t" \
  "ds_write_b128 v8, v[216:219] offset:10240\n\t" \
  "ds_write_b128 v8, v[220:223] offset:11264\n\t" \
  "ds_write_b128 v9, v[176:179] offset:12288\n\t" \
  "ds_write_b128 v9, v[180:183] offset:13312\n\t" \
  "ds_write_b128 v8, v[240:243] offset:12288\n\t" \
  "ds_write_b128 v8, v[244:247] offset:13312\n\t" \
  "ds_write_b128 v9, v[184:187] offset:14336\n\t" \
  "ds_write_b128 v9, v[188:191] offset:15360\n\t" \
  "ds_write_b128 v8, v[248:251] offset:14336\n\t" \
  "ds_write_b128 v8, v[252:255] offset:15360\n\t" \
  "s_mov_b32 s93, 0x05040100\n\t" \
  "s_mov_b32 s94, 0x07060302\n\t" \
  "s_waitcnt vmcnt(0) lgkmcnt(0)\n\t" \
  "v_perm_b32 v252, v228, v47, s93\n\t" \
  "v_perm_b32 v253, v230, v229, s93\n\t" \
  "v_perm_b32 v254, v228, v47, s94\n\t" \
  "v_perm_b32 v255, v230, v229, s94\n\t" \
  "v_perm_b32 v248, v44, v43, s93\n\t" \
  "v_perm_b32 v249, v46, v45, s93\n\t" \
  "v_perm_b32 v250, v44, v43, s94\n\t" \
  "v_perm_b32 v251, v46, v45, s94\n\t" \
  "v_perm_b32 v244, v39, v38, s93\n\t" \
  "v_perm_b32 v245, v42, v41, s93\n\t" \
  "v_perm_b32 v246, v39, v38, s94\n\t" \
  "v_perm_b32 v247, v42, v41, s94\n\t" \
  "v_perm_b32 v240, v35, v34, s93\n\t" \
  "v_perm_b32 v241, v37, v36, s93\n\t" \
  "v_perm_b32 v242, v35, v34, s94\n\t" \
  "v_perm_b32 v243, v37, v36, s94\n\t" \
  "v_perm_b32 v236, v31, v30, s93\n\t" \
  "v_perm_b32 v237, v33, v32, s93\n\t" \
  "v_perm_b32 v238, v31, v30, s94\n\t" \
  "v_perm_b32 v239, v33, v32, s94\n\t" \
  "v_perm_b32 v232, v25, v24, s93\n\t" \
  "v_perm_b32 v233, v27, v26, s93\n\t" \
  "v_perm_b32 v234, v25, v24, s94\n\t" \
  "v_perm_b32 v235, v27, v26, s94\n\t" \
  "v_perm_b32 v228, v21, v20, s93\n\t" \
  "v_perm_b32 v229, v23, v22, s93\n\t" \
  "v_perm_b32 v230, v21, v20, s94\n\t" \
  "v_perm_b32 v231, v23, v22, s94\n\t" \
  "v_perm_b32 v224, v17, v16, s93\n\t" \
  "v_perm_b32 v225, v19, v18, s93\n\t" \
  "v_perm_b32 v226, v17, v16, s94\n\t" \
  "v_perm_b32 v227, v19, v18, s94\n\t" \
  "s_mov_b32 s80, 0\n\t" \
  "s_mov_b32 s49, s66\n\t" \
  "v_lshlrev_b32 v30, 1, v0\n\t" \
  "s_mov_b32 s35, 0\n\t" \
  "s_mov_b32 s94, 6227456\n\t" \
  "L_par%=:\n\t" \
  "buffer_load_dwordx4 v[128:131], v30, s[36:39], s94 offen offset:0 sc1\n\t" \
  "buffer_load_dwordx2 v[132:133], v30, s[36:39], s94 offen offset:16 sc1\n\t" \
  "buffer_load_dwordx4 v[136:139], v30, s[36:39], s94 offen offset:640 sc1\n\t" \
  "buffer_load_dwordx2 v[140:141], v30, s[36:39], s94 offen offset:656 sc1\n\t" \
  "v_and_b32 v52, -4, v2\n\t" \
  "buffer_load_dwordx2 v[52:53], v52, s[36:39], 0 offen sc1\n\t" \
  "s_waitcnt vmcnt(0)\n\t" \
  "v_xor_b32 v129, s49, v129\n\t" \
  "v_xor_b32 v131, s49, v131\n\t" \
  "v_xor_b32 v133, s49, v133\n\t" \
  "v_or3_b32 v129, v129, v131, v133\n\t" \
  "v_xor_b32 v137, s49, v137\n\t" \
  "v_xor_b32 v139, s49, v139\n\t" \
  "v_xor_b32 v141, s49, v141\n\t" \
  "v_or3_b32 v137, v137, v139, v141\n\t" \
  "v_or_b32 v129, v129, v137\n\t" \
  "v_cmp_ne_u32 vcc, 0, v129\n\t" \
  "s_cbranch_vccz L_pare%=\n\t" \
  "s_add_u32 s35, s35, 1\n\t" \
  "s_cmp_lt_u32 s35, s91\n\t" \
  "s_cbranch_scc1 L_par%=\n\t" \
  "s_mov_b32 s91, 1\n\t" \
  "L_pare%=:\n\t" \
  "v_min3_u32 v131, v128, v130, v132\n\t" \
  "v_and_b32 v128, 0xffff, v128\n\t" \
  "v_and_b32 v130, 0xffff, v130\n\t" \
  "v_and_b32 v132, 0xffff, v132\n\t" \
  "v_min3_u32 v129, v128, v130, v132\n\t" \
  "v_lshrrev_b32 v128, 16, v131\n\t" \
  "v_min3_u32 v139, v136, v138, v140\n\t" \
  "v_and_b32 v136, 0xffff, v136\n\t" \
  "v_and_b32 v138, 0xffff, v138\n\t" \
  "v_and_b32 v140, 0xffff, v140\n\t" \
  "v_min3_u32 v137, v136, v138, v140\n\t" \
  "v_lshrrev_b32 v136, 16, v139\n\t" \
  "v_cmp_ge_u32 vcc, v129, v128\n\t" \
  "s_nop 1\n\t" \
  "s_mov_b32 s66, vcc_lo\n\t" \
  "v_cmp_lt_u32 vcc, v137, v136\n\t" \
  "s_nop 1\n\t" \
  "s_mov_b32 s23, vcc_lo\n\t" \
  "s_mov_b32 s21, vcc_hi\n\t" \
  "s_bitcmp0_b32 s66, 2\n\t" \
  "s_cselect_b32 s93, 8192, 0\n\t" \
  "s_add_u32 s93, s93, 6242304\n\t" \
  "s_add_u32 s93, s93, s71\n\t" \
  "s_add_u32 s28, s36, s93\n\t" \
  "s_addc_u32 s29, s37, 0\n\t" \
  "s_cmp_eq_u32 s64, 3\n\t" \
  "s_cselect_b32 s30, 8, 0\n\t" \
  "s_add_u32 s52, s72, 0\n\t" \
  "s_and_b32 s52, s52, 7\n\t" \
  "s_lshl_b32 s52, s52, 10\n\t" \
  "s_add_u32 s53, s72, 1\n\t" \
  "s_and_b32 s53, s53, 7\n\t" \
  "s_lshl_b32 s53, s53, 10\n\t" \
  "s_add_u32 s54, s72, 2\n\t" \
  "s_and_b32 s54, s54, 7\n\t" \
  "s_lshl_b32 s54, s54, 10\n\t" \
  "s_add_u32 s55, s72, 3\n\t" \
  "s_and_b32 s55, s55, 7\n\t" \
  "s_lshl_b32 s55, s55, 10\n\t" \
  "s_add_u32 s56, s72, 4\n\t" \
  "s_and_b32 s56, s56, 7\n\t" \
  "s_lshl_b32 s56, s56, 10\n\t" \
  "s_add_u32 s57, s72, 5\n\t" \
  "s_and_b32 s57, s57, 7\n\t" \
  "s_lshl_b32 s57, s57, 10\n\t" \
  "s_add_u32 s58, s72, 6\n\t" \
  "s_and_b32 s58, s58, 7\n\t" \
  "s_lshl_b32 s58, s58, 10\n\t" \
  "s_add_u32 s59, s72, 7\n\t" \
  "s_and_b32 s59, s59, 7\n\t" \
  "s_lshl_b32 s59, s59, 10\n\t" \
  "s_and_b32 s72, s72, -8\n\t" \
  "s_cmp_eq_u32 s64, 1\n\t" \
  "s_cselect_b32 s93, 262144, 0\n\t" \
  "s_xor_b32 s34, s70, s93\n\t" \
  "s_cmp_eq_u32 s64, 1\n\t" \
  "s_cselect_b32 s50, 0x40000000, s71\n\t" \
  "s_add_u32 s50, s50, 6225920\n\t" \
  "s_add_u32 s72, s72, 6225920\n\t" \
  "s_add_u32 s65, s65, 6225920\n\t" \
  "s_add_u32 s69, s69, 6291456\n\t" \
  "s_add_u32 s34, s34, 6291456\n\t" \
  "s_add_u32 s70, s70, 6291456\n\t" \
  "s_lshr_b32 s93, s75, 2\n\t" \
  "s_add_u32 s70, s70, s93\n\t" \
  "s_mov_b32 s62, 8192\n\t" \
  "s_mov_b32 s63, s39\n\t" \
  "v_mov_b32 v128, -1\n\t" \
  "v_mov_b32 v129, -1\n\t" \
  "v_mov_b32 v130, -1\n\t" \
  "v_mov_b32 v131, -1\n\t" \
  "s_add_u32 s93, s80, 0\n\t" \
  "s_lshr_b32 s94, s93, 2\n\t" \
  "s_add_u32 s94, s94, 3\n\t" \
  "s_lshr_b32 s94, s66, s94\n\t" \
  "s_and_b32 s94, s94, 1\n\t" \
  "s_lshl_b32 s94, s94, 13\n\t" \
  "s_lshl_b32 s83, s93, 19\n\t" \
  "s_or_b32 s83, s83, s94\n\t" \
  "s_add_u32 s83, s83, s70\n\t" \
  "buffer_store_dwordx4 v[128:131], v0, s[36:39], s83 offen sc1\n\t" \
  "s_add_u32 s93, s80, 1\n\t" \
  "s_lshr_b32 s94, s93, 5\n\t" \
  "s_sub_u32 s94, 2, s94\n\t" \
  "s_lshr_b32 s94, s66, s94\n\t" \
  "s_and_b32 s94, s94, 1\n\t" \
  "s_lshl_b32 s94, s94, 13\n\t" \
  "s_lshl_b32 s93, s93, 16\n\t" \
  "s_sub_u32 s86, s94, s93\n\t" \
  "s_add_u32 s86, s86, s65\n\t" \
  "buffer_store_dwordx4 v[128:131], v11, s[36:39], s86 offen sc1\n\t" \
  "s_add_u32 s93, s80, 1\n\t" \
  "s_lshr_b32 s94, s93, 2\n\t" \
  "s_add_u32 s94, s94, 3\n\t" \
  "s_lshr_b32 s94, s66, s94\n\t" \
  "s_and_b32 s94, s94, 1\n\t" \
  "s_lshl_b32 s94, s94, 13\n\t" \
  "s_lshl_b32 s83, s93, 19\n\t" \
  "s_or_b32 s83, s83, s94\n\t" \
  "s_add_u32 s83, s83, s70\n\t" \
  "buffer_store_dwordx4 v[128:131], v0, s[36:39], s83 offen sc1\n\t" \
  "s_add_u32 s93, s80, 2\n\t" \
  "s_lshr_b32 s94, s93, 5\n\t" \
  "s_sub_u32 s94, 2, s94\n\t" \
  "s_lshr_b32 s94, s66, s94\n\t" \
  "s_and_b32 s94, s94, 1\n\t" \
  "s_lshl_b32 s94, s94, 13\n\t" \
  "s_lshl_b32 s93, s93, 16\n\t" \
  "s_sub_u32 s86, s94, s93\n\t" \
  "s_add_u32 s86, s86, s65\n\t" \
  "buffer_store_dwordx4 v[128:131], v11, s[36:39], s86 offen sc1\n\t" \
  "s_waitcnt vmcnt(0)\n\t" \
  "s_barrier\n\t" \
  "s_lshr_b32 s93, s71, 3\n\t" \
  "s_add_u32 s93, s93, 6226432\n\t" \
  "v_mov_b32 v30, 0\n\t" \
  "v_mov_b32 v31, s49\n\t" \
  "buffer_store_dwordx2 v[30:31], off, s[36:39], s93 sc1\n\t" \
  "s_mov_b32 s35, 0\n\t" \
  "L_k0a%=:\n\t" \
  "v_cmp_eq_u32 vcc, s49, v53\n\t" \
  "v_and_b32 v53, 1, v2\n\t" \
  "s_nop 0\n\t" \
  "v_cndmask_b32 v52, -1, v52, vcc\n\t" \
  "v_and_b32 v53, 1, v2\n\t" \
  "v_lshl_add_u32 v52, v52, 1, v53\n\t" \
  "s_nop 1\n\t" \
  "v_min_i32_dpp v52, v52, v52 row_shr:1 row_mask:0xf bank_mask:0xf\n\t" \
  "s_nop 1\n\t" \
  "v_min_i32_dpp v52, v52, v52 row_shr:2 row_mask:0xf bank_mask:0xf\n\t" \
  "s_nop 1\n\t" \
  "v_min_i32_dpp v52, v52, v52 row_shr:4 row_mask:0xf bank_mask:0xf\n\t" \
  "s_nop 1\n\t" \
  "v_readlane_b32 s51, v52, 7\n\t" \
  "s_nop 1\n\t" \
  "s_add_i32 s51, s51, 1\n\t" \
  "s_add_i32 s93, s80, 1\n\t" \
  "s_min_i32 s93, s93, 63\n\t" \
  "s_cmp_ge_i32 s51, s93\n\t" \
  "s_cbranch_scc1 L_k0e%=\n\t" \
  "s_add_u32 s35, s35, 1\n\t" \
  "s_cmp_lt_u32 s35, s91\n\t" \
  "s_cbranch_scc0 L_k0d%=\n\t" \
  "s_sleep 2\n\t" \
  "v_and_b32 v52, -4, v2\n\t" \
  "buffer_load_dwordx2 v[52:53], v52, s[36:39], 0 offen sc1\n\t" \
  "s_waitcnt vmcnt(0)\n\t" \
  "s_branch L_k0a%=\n\t" \
  "L_k0d%=:\n\t" \
  "s_mov_b32 s91, 1\n\t" \
  "L_k0e%=:\n\t" \
  "s_add_u32 s93, s80, 0\n\t" \
  "s_lshr_b32 s94, s93, 2\n\t" \
  "s_lshr_b32 s94, s23, s94\n\t" \
  "s_and_b32 s94, s94, 1\n\t" \
  "s_lshl_b32 s94, s94, 13\n\t" \
  "s_lshl_b32 s87, s93, 19\n\t" \
  "s_or_b32 s87, s87, s94\n\t" \
  "s_add_u32 s87, s87, s73\n\t" \
  "s_lshr_b32 s94, s93, 2\n\t" \
  "s_add_u32 s94, s94, 16\n\t" \
  "s_lshr_b32 s94, s23, s94\n\t" \
  "s_and_b32 s94, s94, 1\n\t" \
  "s_lshl_b32 s94, s94, 13\n\t" \
  "s_andn2_b32 s88, s87, 0x2000\n\t" \
  "s_or_b32 s88, s88, s94\n\t" \
  "s_add_u32 s88, s88, 33554432\n\t" \
  "s_mov_b32 m0, s95\n\t" \
  "s_nop 0\n\t" \
  "buffer_load_dwordx4 v0, s[40:43], s87 offen sc1 lds\n\t" \
  "buffer_load_dwordx4 v0, s[40:43], s87 offen offset:1024 sc1 lds\n\t" \
  "s_mov_b32 m0, s96\n\t" \
  "s_nop 0\n\t" \
  "buffer_load_dwordx4 v0, s[40:43], s88 offen sc1 lds\n\t" \
  "buffer_load_dwordx4 v0, s[40:43], s88 offen offset:1024 sc1 lds\n\t" \
  "s_mov_b32 s35, 0\n\t" \
  "s_mov_b32 s94, 6226432\n\t" \
  "L_rdy%=:\n\t" \
  "buffer_load_dwordx2 v[30:31], v0, s[36:39], s94 offen sc1\n\t" \
  "s_waitcnt vmcnt(0)\n\t" \
  "v_cmp_ne_u32 vcc, s49, v31\n\t" \
  "s_cbranch_vccz L_rdye%=\n\t" \
  "s_add_u32 s35, s35, 1\n\t" \
  "s_cmp_lt_u32 s35, s91\n\t" \
  "s_cbranch_scc1 L_rdy%=\n\t" \
  "s_mov_b32 s91, 1\n\t" \
  "L_rdye%=:\n\t" \
  "s_barrier\n\t" \
  "v_mov_b32 v16, 0x3f803f80\n\t" \
  "v_mov_b32 v17, 0x3f803f80\n\t" \
  "v_mov_b32 v18, 0x3f803f80\n\t" \
  "v_mov_b32 v19, 0x3f803f80\n\t" \
  "v_mov_b32 v20, 0\n\t" \
  "v_mov_b32 v21, 0\n\t" \
  "v_mov_b32 v22, 0\n\t" \
  "v_mov_b32 v23, 0\n\t" \
  "v_mov_b32 v24, 0\n\t" \
  "v_mov_b32 v25, 0\n\t" \
  "v_mov_b32 v26, 0\n\t" \
  "v_mov_b32 v27, 0\n\t" \
  "s_mov_b32 s80, 0\n\t" \
  "s_mov_b32 s98, 0\n\t" \
  "s_mov_b32 s99, 0\n\t" \
  "s_add_u32 s93, s80, 0\n\t" \
  "s_lshr_b32 s94, s93, 2\n\t" \
  "s_add_u32 s94, s94, 3\n\t" \
  "s_lshr_b32 s94, s66, s94\n\t" \
  "s_and_b32 s94, s94, 1\n\t" \
  "s_lshl_b32 s94, s94, 13\n\t" \
  "s_lshl_b32 s27, s93, 19\n\t" \
  "s_or_b32 s27, s27, s94\n\t" \
  "s_add_u32 s93, s80, 1\n\t" \
  "s_lshr_b32 s94, s93, 2\n\t" \
  "s_add_u32 s94, s94, 3\n\t" \
  "s_lshr_b32 s94, s66, s94\n\t" \
  "s_and_b32 s94, s94, 1\n\t" \
  "s_lshl_b32 s94, s94, 13\n\t" \
  "s_lshl_b32 s97, s93, 19\n\t" \
  "s_or_b32 s97, s97, s94\n\t" \
  "s_add_u32 s93, s80, 1\n\t" \
  "s_lshr_b32 s94, s93, 5\n\t" \
  "s_sub_u32 s94, 2, s94\n\t" \
  "s_lshr_b32 s94, s66, s94\n\t" \
  "s_and_b32 s94, s94, 1\n\t" \
  "s_lshl_b32 s94, s94, 13\n\t" \
  "s_lshl_b32 s93, s93, 16\n\t" \
  "s_sub_u32 s22, s94, s93\n\t" \
  "s_add_u32 s81, s27, s69\n\t" \
  "s_add_u32 s82, s27, s34\n\t" \
  "s_mov_b32 s27, s97\n\t" \
  "s_add_u32 s93, s80, 2\n\t" \
  "s_lshr_b32 s94, s93, 2\n\t" \
  "s_add_u32 s94, s94, 3\n\t" \
  "s_lshr_b32 s94, s66, s94\n\t" \
  "s_and_b32 s94, s94, 1\n\t" \
  "s_lshl_b32 s94, s94, 13\n\t" \
  "s_lshl_b32 s97, s93, 19\n\t" \
  "s_or_b32 s97, s97, s94\n\t" \
  "s_add_u32 s83, s97, s70\n\t" \
  "s_add_u32 s93, s80, 0\n\t" \
  "s_lshr_b32 s94, s93, 2\n\t" \
  "s_lshr_b32 s94, s21, s94\n\t" \
  "s_and_b32 s94, s94, 1\n\t" \
  "s_lshl_b32 s94, s94, 13\n\t" \
  "s_lshl_b32 s89, s93, 19\n\t" \
  "s_or_b32 s89, s89, s94\n\t" \
  "s_add_u32 s89, s89, s74\n\t" \
  "s_xor_b32 s90, s89, 0x40000000\n\t" \
  "s_add_u32 s93, s80, 1\n\t" \
  "s_lshr_b32 s94, s93, 2\n\t" \
  "s_lshr_b32 s94, s23, s94\n\t" \
  "s_and_b32 s94, s94, 1\n\t" \
  "s_lshl_b32 s94, s94, 13\n\t" \
  "s_lshl_b32 s87, s93, 19\n\t" \
  "s_or_b32 s87, s87, s94\n\t" \
  "s_add_u32 s87, s87, s73\n\t" \
  "s_lshr_b32 s94, s93, 2\n\t" \
  "s_add_u32 s94, s94, 16\n\t" \
  "s_lshr_b32 s94, s23, s94\n\t" \
  "s_and_b32 s94, s94, 1\n\t" \
  "s_lshl_b32 s94, s94, 13\n\t" \
  "s_andn2_b32 s88, s87, 0x2000\n\t" \
  "s_or_b32 s88, s88, s94\n\t" \
  "s_add_u32 s88, s88, 33554432\n\t" \
  "s_add_u32 s85, s22, s72\n\t" \
  "s_add_u32 s84, s22, s50\n\t" \
  "s_add_u32 s93, s80, 2\n\t" \
  "s_lshr_b32 s94, s93, 5\n\t" \
  "s_sub_u32 s94, 2, s94\n\t" \
  "s_lshr_b32 s94, s66, s94\n\t" \
  "s_and_b32 s94, s94, 1\n\t" \
  "s_lshl_b32 s94, s94, 13\n\t" \
  "s_lshl_b32 s93, s93, 16\n\t" \
  "s_sub_u32 s22, s94, s93\n\t" \
  "s_add_u32 s86, s22, s65\n\t" \
  "ds_read_b128 v[128:131], v9 offset:0\n\t" \
  "ds_read_b128 v[132:135], v9 offset:1024\n\t" \
  "ds_read_b128 v[136:139], v9 offset:2048\n\t" \
  "ds_read_b128 v[140:143], v9 offset:3072\n\t" \
  "ds_read_b128 v[144:147], v9 offset:4096\n\t" \
  "ds_read_b128 v[148:151], v9 offset:5120\n\t" \
  "ds_read_b128 v[152:155], v9 offset:6144\n\t" \
  "ds_read_b128 v[156:159], v9 offset:7168\n\t" \
  "ds_read_b128 v[208:211], v9 offset:8192\n\t" \
  "ds_read_b128 v[212:215], v9 offset:9216\n\t" \
  "ds_read_b128 v[216:219], v9 offset:10240\n\t" \
  "ds_read_b128 v[220:223], v9 offset:11264\n\t" \
  "ds_read_b128 v[192:195], v9 offset:12288\n\t" \
  "ds_read_b128 v[196:199], v9 offset:13312\n\t" \
  "ds_read_b128 v[200:203], v9 offset:14336\n\t" \
  "ds_read_b128 v[204:207], v9 offset:15360\n\t" \
  "ds_read_b128 v[160:163], v12 offset:0\n\t" \
  "ds_read_b128 v[164:167], v12 offset:1024\n\t" \
  "ds_read_b128 v[168:171], v12 offset:2048\n\t" \
  "ds_read_b128 v[172:175], v12 offset:3072\n\t" \
  "ds_read_b128 v[176:179], v12 offset:4096\n\t" \
  "ds_read_b128 v[180:183], v12 offset:5120\n\t" \
  "ds_read_b128 v[184:187], v12 offset:6144\n\t" \
  "ds_read_b128 v[188:191], v12 offset:7168\n\t" \
  "v_mov_b64 v[40:41], 0\n\t" \
  "v_mov_b64 v[42:43], 0\n\t" \
  "v_mov_b64 v[44:45], 0\n\t" \
  "v_mov_b64 v[46:47], 0\n\t" \
  "v_mov_b64 v[48:49], 0\n\t" \
  "v_mov_b64 v[50:51], 0\n\t" \
  "v_mov_b64 v[52:53], 0\n\t" \
  "v_mov_b64 v[54:55], 0\n\t" \
  "v_mov_b64 v[56:57], 0\n\t" \
  "v_mov_b64 v[58:59], 0\n\t" \
  "v_mov_b64 v[60:61], 0\n\t" \
  "v_mov_b64 v[62:63], 0\n\t" \
  "s_waitcnt lgkmcnt(0)\n\t" \
  "s_waitcnt lgkmcnt(0)\n\t" \
  "s_mov_b32 s35, 0\n\t" \
  "s_mov_b32 m0, s95\n\t" \
  "L_top%=:\n\t" \
  "s_waitcnt vmcnt(7)\n\t" \
  "v_max3_u32 v30, v224, v225, v226\n\t" \
  "v_max_u32 v30, v30, v227\n\t" \
  "s_waitcnt vmcnt(6)\n\t" \
  "v_max3_u32 v30, v30, v228, v229\n\t" \
  "v_max3_u32 v30, v30, v230, v231\n\t" \
  "v_cmp_eq_u32 vcc, -1, v30\n\t" \
  "s_cbranch_vccnz L_ra0%=\n\t" \
  "L_ok0%=:\n\t" \
  "v_smfmac_f32_16x16x64_bf16 v[48:51], v[224:227], a[0:7], v1\n\t" \
  "ds_read_b128 v[32:35], v14 offset:0\n\t" \
  "v_smfmac_f32_16x16x64_bf16 v[52:55], v[224:227], a[64:71], v1\n\t" \
  "ds_read_b128 v[36:39], v14 offset:1024\n\t" \
  "v_smfmac_f32_16x16x64_bf16 v[56:59], v[224:227], a[128:135], v1\n\t" \
  "s_waitcnt vmcnt(5)\n\t" \
  "v_smfmac_f32_16x16x64_bf16 v[60:63], v[224:227], a[192:199], v1\n\t" \
  "v_max3_u32 v30, v232, v233, v234\n\t" \
  "v_smfmac_f32_16x16x64_bf16 v[48:51], v[228:231], a[8:15], v1\n\t" \
  "v_max_u32 v30, v30, v235\n\t" \
  "v_smfmac_f32_16x16x64_bf16 v[52:55], v[228:231], a[72:79], v1\n\t" \
  "s_waitcnt vmcnt(4)\n\t" \
  "v_max3_u32 v30, v30, v236, v237\n\t" \
  "v_smfmac_f32_16x16x64_bf16 v[56:59], v[228:231], a[136:143], v1\n\t" \
  "v_max3_u32 v30, v30, v238, v239\n\t" \
  "v_cmp_eq_u32 vcc, -1, v30\n\t" \
  "v_smfmac_f32_16x16x64_bf16 v[60:63], v[228:231], a[200:207], v1\n\t" \
  "s_cbranch_vccnz L_ra1%=\n\t" \
  "L_ok1%=:\n\t" \
  "v_smfmac_f32_16x16x64_bf16 v[48:51], v[232:235], a[16:23], v1\n\t" \
  "buffer_load_dwordx4 v0, s[40:43], s87 offen sc1 lds\n\t" \
  "v_smfmac_f32_16x16x64_bf16 v[52:55], v[232:235], a[80:87], v1\n\t" \
  "buffer_load_dwordx4 v0, s[40:43], s87 offen offset:1024 sc1 lds\n\t" \
  "v_smfmac_f32_16x16x64_bf16 v[56:59], v[232:235], a[144:151], v1\n\t" \
  "s_waitcnt vmcnt(5)\n\t" \
  "v_smfmac_f32_16x16x64_bf16 v[60:63], v[232:235], a[208:215], v1\n\t" \
  "v_max3_u32 v30, v240, v241, v242\n\t" \
  "v_smfmac_f32_16x16x64_bf16 v[48:51], v[236:239], a[24:31], v1\n\t" \
  "v_max_u32 v30, v30, v243\n\t" \
  "v_smfmac_f32_16x16x64_bf16 v[52:55], v[236:239], a[88:95], v1\n\t" \
  "s_waitcnt vmcnt(4)\n\t" \
  "v_max3_u32 v30, v30, v244, v245\n\t" \
  "v_smfmac_f32_16x16x64_bf16 v[56:59], v[236:239], a[152:159], v1\n\t" \
  "v_max3_u32 v30, v30, v246, v247\n\t" \
  "v_cmp_eq_u32 vcc, -1, v30\n\t" \
  "v_smfmac_f32_16x16x64_bf16 v[60:63], v[236:239], a[216:223], v1\n\t" \
  "s_cbranch_vccnz L_ra2%=\n\t" \
  "L_ok2%=:\n\t" \
  "v_smfmac_f32_16x16x64_bf16 v[48:51], v[240:243], a[32:39], v1\n\t" \
  "s_mov_b32 m0, s96\n\t" \
  "v_smfmac_f32_16x16x64_bf16 v[52:55], v[240:243], a[96:103], v1\n\t" \
  "s_waitcnt lgkmcnt(0)\n\t" \
  "v_smfmac_f32_16x16x64_bf16 v[56:59], v[240:243], a[160:167], v1\n\t" \
  "s_waitcnt vmcnt(3)\n\t" \
  "v_smfmac_f32_16x16x64_bf16 v[60:63], v[240:243], a[224:231], v1\n\t" \
  "v_max3_u32 v30, v248, v249, v250\n\t" \
  "v_smfmac_f32_16x16x64_bf16 v[48:51], v[244:247], a[40:47], v1\n\t" \
  "v_max_u32 v30, v30, v251\n\t" \
  "v_smfmac_f32_16x16x64_bf16 v[52:55], v[244:247], a[104:111], v1\n\t" \
  "s_waitcnt vmcnt(2)\n\t" \
  "v_max3_u32 v30, v30, v252, v253\n\t" \
  "v_smfmac_f32_16x16x64_bf16 v[56:59], v[244:247], a[168:175], v1\n\t" \
  "v_max3_u32 v30, v30, v254, v255\n\t" \
  "v_cmp_eq_u32 vcc, -1, v30\n\t" \
  "v_smfmac_f32_16x16x64_bf16 v[60:63], v[244:247], a[232:239], v1\n\t" \
  "s_cbranch_vccnz L_ra3%=\n\t" \
  "L_ok3%=:\n\t" \
  "v_smfmac_f32_16x16x64_bf16 v[48:51], v[248:251], a[48:55], v1\n\t" \
  "buffer_load_dwordx4 v0, s[40:43], s88 offen sc1 lds\n\t" \
  "v_smfmac_f32_16x16x64_bf16 v[52:55], v[248:251], a[112:119], v1\n\t" \
  "v_smfmac_f32_16x16x64_bf16 v[56:59], v[248:251], a[176:183], v1\n\t" \
  "v_smfmac_f32_16x16x64_bf16 v[60:63], v[248:251], a[240:247], v1\n\t" \
  "v_smfmac_f32_16x16x64_bf16 v[48:51], v[252:255], a[56:63], v1\n\t" \
  "buffer_load_dwordx4 v0, s[40:43], s88 offen offset:1024 sc1 lds\n\t" \
  "v_smfmac_f32_16x16x64_bf16 v[52:55], v[252:255], a[120:127], v1\n\t" \
  "v_smfmac_f32_16x16x64_bf16 v[56:59], v[252:255], a[184:191], v1\n\t" \
  "v_smfmac_f32_16x16x64_bf16 v[60:63], v[252:255], a[248:255], v1\n\t" \
  "v_smfmac_f32_16x16x64_bf16 v[40:43], v[240:243], v[128:135], v1\n\t" \
  "v_smfmac_f32_16x16x64_bf16 v[44:47], v[240:243], v[136:143], v1\n\t" \
  "s_nop 2\n\t" \
  "v_add_f32 v48, v48, v50\n\t" \
  "v_add_f32 v49, v49, v51\n\t" \
  "v_add_f32 v52, v52, v54\n\t" \
  "v_add_f32 v53, v53, v55\n\t" \
  "v_add_f32 v56, v56, v58\n\t" \
  "v_add_f32 v57, v57, v59\n\t" \
  "v_add_f32 v60, v60, v62\n\t" \
  "v_add_f32 v61, v61, v63\n\t" \
  "v_cvt_pk_bf16_f32 v56, v56, v57\n\t" \
  "v_cvt_pk_bf16_f32 v57, v48, v49\n\t" \
  "v_cvt_pk_bf16_f32 v60, v60, v61\n\t" \
  "v_cvt_pk_bf16_f32 v61, v52, v53\n\t" \
  "ds_write2st64_b64 v3, v[56:57], v[60:61] offset0:0 offset1:1\n\t" \
  "v_smfmac_f32_16x16x64_bf16 v[40:43], v[244:247], v[144:151], v1\n\t" \
  "v_smfmac_f32_16x16x64_bf16 v[44:47], v[244:247], v[152:159], v1\n\t" \
  "v_smfmac_f32_16x16x64_bf16 v[40:43], v[248:251], v[208:215], v1\n\t" \
  "v_smfmac_f32_16x16x64_bf16 v[44:47], v[248:251], v[216:223], v1\n\t" \
  "s_waitcnt lgkmcnt(0)\n\t" \
  "s_barrier\n\t" \
  "ds_read_b64_tr_b16 v[128:129], v4 offset:0\n\t" \
  "ds_read_b64_tr_b16 v[130:131], v4 offset:512\n\t" \
  "v_smfmac_f32_16x16x64_bf16 v[40:43], v[252:255], v[192:199], v1\n\t" \
  "ds_read_b64_tr_b16 v[132:133], v4 offset:1024\n\t" \
  "ds_read_b64_tr_b16 v[134:135], v4 offset:1536\n\t" \
  "v_smfmac_f32_16x16x64_bf16 v[44:47], v[252:255], v[200:207], v1\n\t" \
  "ds_read_b64_tr_b16 v[136:137], v4 offset:2048\n\t" \
  "ds_read_b64_tr_b16 v[138:139], v4 offset:2560\n\t" \
  "v_smfmac_f32_16x16x64_bf16 v[40:43], v[224:227], v[64:71], v1\n\t" \
  "ds_read_b64_tr_b16 v[140:141], v4 offset:3072\n\t" \
  "ds_read_b64_tr_b16 v[142:143], v4 offset:3584\n\t" \
  "v_smfmac_f32_16x16x64_bf16 v[44:47], v[224:227], v[72:79], v1\n\t" \
  "ds_read_b64_tr_b16 v[244:245], v13 offset:0\n\t" \
  "v_smfmac_f32_16x16x64_bf16 v[40:43], v[228:231], v[80:87], v1\n\t" \
  "ds_read_b64_tr_b16 v[246:247], v13 offset:512\n\t" \
  "v_smfmac_f32_16x16x64_bf16 v[44:47], v[228:231], v[88:95], v1\n\t" \
  "s_waitcnt lgkmcnt(8)\n\t" \
  "v_mfma_f32_16x16x32_bf16 v[48:51], v[160:163], v[128:131], 0\n\t" \
  "ds_read_b128 v[144:147], v8 offset:0\n\t" \
  "v_mov_b32 v240, -1\n\t" \
  "v_mfma_f32_16x16x32_bf16 v[52:55], v[176:179], v[128:131], 0\n\t" \
  "ds_read_b128 v[148:151], v8 offset:1024\n\t" \
  "s_waitcnt lgkmcnt(8)\n\t" \
  "v_mfma_f32_16x16x32_bf16 v[48:51], v[164:167], v[132:135], v[48:51]\n\t" \
  "ds_read_b128 v[152:155], v8 offset:2048\n\t" \
  "v_mov_b32 v241, -1\n\t" \
  "v_mfma_f32_16x16x32_bf16 v[52:55], v[180:183], v[132:135], v[52:55]\n\t" \
  "ds_read_b128 v[156:159], v8 offset:3072\n\t" \
  "s_waitcnt lgkmcnt(4)\n\t" \
  "v_mfma_f32_16x16x32_bf16 v[48:51], v[168:171], v[136:139], v[48:51]\n\t" \
  "ds_read_b128 v[208:211], v8 offset:4096\n\t" \
  "buffer_store_dwordx4 v[244:247], v0, s[44:47], s89 offen sc1\n\t" \
  "v_mfma_f32_16x16x32_bf16 v[52:55], v[184:187], v[136:139], v[52:55]\n\t" \
  "ds_read_b128 v[212:215], v8 offset:5120\n\t" \
  "v_mov_b32 v242, -1\n\t" \
  "v_mfma_f32_16x16x32_bf16 v[48:51], v[172:175], v[140:143], v[48:51]\n\t" \
  "ds_read_b128 v[216:219], v8 offset:6144\n\t" \
  "v_mov_b32 v243, -1\n\t" \
  "v_mfma_f32_16x16x32_bf16 v[52:55], v[188:191], v[140:143], v[52:55]\n\t" \
  "ds_read_b128 v[220:223], v8 offset:7168\n\t" \
  "buffer_store_dwordx4 v[240:243], v0, s[36:39], s83 offen sc1\n\t" \
  "v_mfma_f32_16x16x32_bf16 v[56:59], v[128:131], v[128:131], 0\n\t" \
  "ds_read_b128 v[192:195], v8 offset:8192\n\t" \
  "ds_read_b128 v[196:199], v8 offset:9216\n\t" \
  "v_mfma_f32_16x16x32_bf16 v[56:59], v[132:135], v[132:135], v[56:59]\n\t" \
  "ds_read_b128 v[200:203], v8 offset:10240\n\t" \
  "v_mfma_f32_16x16x32_bf16 v[56:59], v[136:139], v[136:139], v[56:59]\n\t" \
  "ds_read_b128 v[204:207], v8 offset:11264\n\t" \
  "v_max3_f32 v30, v48, v49, v50\n\t" \
  "v_mfma_f32_16x16x32_bf16 v[56:59], v[140:143], v[140:143], v[56:59]\n\t" \
  "v_max3_f32 v31, v51, v52, v53\n\t" \
  "v_smfmac_f32_16x16x64_bf16 v[40:43], v[232:235], v[96:103], v1\n\t" \
  "v_max3_f32 v30, v30, v54, v55\n\t" \
  "v_smfmac_f32_16x16x64_bf16 v[44:47], v[232:235], v[104:111], v1\n\t" \
  "v_max_f32 v30, v30, v31\n\t" \
  "v_smfmac_f32_16x16x64_bf16 v[40:43], v[236:239], v[112:119], v1\n\t" \
  "v_smfmac_f32_16x16x64_bf16 v[44:47], v[236:239], v[120:127], v1\n\t" \
  "s_nop 0\n\t" \
  "v_max3_f32 v31, v30, v56, v57\n\t" \
  "v_max3_f32 v30, v30, v56, v57\n\t" \
  "ds_read_b128 v[160:163], v8 offset:12288\n\t" \
  "ds_read_b128 v[164:167], v8 offset:13312\n\t" \
  "v_permlane16_swap_b32 v30, v31\n\t" \
  "v_max_f32 v30, v30, v31\n\t" \
  "v_mov_b32 v31, v30\n\t" \
  "v_pk_add_f32 v[40:41], v[40:41], v[42:43]\n\t" \
  "v_pk_add_f32 v[44:45], v[44:45], v[46:47]\n\t" \
  "v_permlane32_swap_b32 v30, v31\n\t" \
  "v_max_f32 v30, v30, v31\n\t" \
  "v_mul_f32 v30, s78, v30\n\t" \
  "v_pk_fma_f32 v[56:57], v[56:57], s[76:77], v[30:31] op_sel_hi:[1,1,0]\n\t" \
  "v_pk_fma_f32 v[48:49], v[48:49], s[76:77], v[30:31] op_sel_hi:[1,1,0]\n\t" \
  "v_pk_fma_f32 v[50:51], v[50:51], s[76:77], v[30:31] op_sel_hi:[1,1,0]\n\t" \
  "v_pk_fma_f32 v[52:53], v[52:53], s[76:77], v[30:31] op_sel_hi:[1,1,0]\n\t" \
  "v_pk_fma_f32 v[54:55], v[54:55], s[76:77], v[30:31] op_sel_hi:[1,1,0]\n\t" \
  "v_exp_f32 v56, v56\n\t" \
  "v_exp_f32 v57, v57\n\t" \
  "v_cvt_pk_bf16_f32 v20, v40, v41\n\t" \
  "v_cvt_pk_bf16_f32 v22, v44, v45\n\t" \
  "v_cvt_pk_bf16_f32 v24, v56, v57\n\t" \
  "v_exp_f32 v48, v48\n\t" \
  "v_exp_f32 v49, v49\n\t" \
  "v_mfma_f32_16x16x32_bf16 v[56:59], v[16:19], v[24:27], 0\n\t" \
  "buffer_store_dwordx4 v[20:23], v0, s[44:47], s90 offen sc1\n\t" \
  "v_exp_f32 v50, v50\n\t" \
  "v_cvt_pk_bf16_f32 v60, v48, v49\n\t" \
  "v_mfma_f32_16x16x32_bf16 v[224:227], v[20:23], v[24:27], 0\n\t" \
  "v_exp_f32 v51, v51\n\t" \
  "v_exp_f32 v52, v52\n\t" \
  "v_mfma_f32_16x16x32_bf16 v[228:231], v[22:25], v[24:27], 0\n\t" \
  "v_exp_f32 v53, v53\n\t" \
  "v_cvt_pk_bf16_f32 v61, v50, v51\n\t" \
  "v_exp_f32 v54, v54\n\t" \
  "v_exp_f32 v55, v55\n\t" \
  "v_cvt_pk_bf16_f32 v62, v52, v53\n\t" \
  "v_cvt_pk_bf16_f32 v63, v54, v55\n\t" \
  "ds_read_b128 v[168:171], v8 offset:14336\n\t" \
  "ds_read_b128 v[172:175], v8 offset:15360\n\t" \
  "v_mfma_f32_16x16x32_bf16 v[56:59], v[16:19], v[60:63], v[56:59]\n\t" \
  "v_mov_b32 v240, 0\n\t" \
  "v_mov_b32 v241, 0\n\t" \
  "v_mfma_f32_16x16x32_bf16 v[224:227], v[32:35], v[60:63], v[224:227]\n\t" \
  "v_mov_b32 v242, 0\n\t" \
  "v_mov_b32 v243, 0\n\t" \
  "v_mfma_f32_16x16x32_bf16 v[228:231], v[36:39], v[60:63], v[228:231]\n\t" \
  "v_mov_b32 v244, 0\n\t" \
  "v_mov_b32 v245, 0\n\t" \
  "v_rcp_f32 v30, v56\n\t" \
  "v_mov_b32 v246, 0\n\t" \
  "v_mov_b32 v247, 0\n\t" \
  "v_pk_mul_f32 v[224:225], v[224:225], v[30:31] op_sel_hi:[1,0]\n\t" \
  "v_pk_mul_f32 v[226:227], v[226:227], v[30:31] op_sel_hi:[1,0]\n\t" \
  "v_cvt_pk_bf16_f32 v60, v224, v225\n\t" \
  "v_pk_mul_f32 v[228:229], v[228:229], v[30:31] op_sel_hi:[1,0]\n\t" \
  "v_pk_mul_f32 v[230:231], v[230:231], v[30:31] op_sel_hi:[1,0]\n\t" \
  "v_cvt_pk_bf16_f32 v61, v226, v227\n\t" \
  "v_cvt_pk_bf16_f32 v62, v228, v229\n\t" \
  "v_cvt_pk_bf16_f32 v63, v230, v231\n\t" \
  "ds_write_b128 v5, v[60:63]\n\t" \
  "v_mov_b32 v248, 0\n\t" \
  "v_mov_b32 v249, 0\n\t" \
  "v_mov_b32 v250, 0\n\t" \
  "v_mov_b32 v251, 0\n\t" \
  "v_mov_b32 v252, 0\n\t" \
  "v_mov_b32 v253, 0\n\t" \
  "v_mov_b32 v254, 0\n\t" \
  "v_mov_b32 v255, 0\n\t" \
  "s_cmp_eq_u32 s80, 63\n\t" \
  "s_waitcnt lgkmcnt(0)\n\t" \
  "s_barrier\n\t" \
  "ds_read_b128 v[232:235], v6 offset:0\n\t" \
  "ds_read_b128 v[236:239], v7 offset:0\n\t" \
  "s_cbranch_scc1 L_done%=\n\t" \
  "s_waitcnt lgkmcnt(1)\n\t" \
  "v_smfmac_f32_16x16x64_bf16 v[240:243], v[232:235], v[144:151], v1\n\t" \
  "s_waitcnt lgkmcnt(0)\n\t" \
  "v_smfmac_f32_16x16x64_bf16 v[240:243], v[236:239], v[152:159], v1\n\t" \
  "v_smfmac_f32_16x16x64_bf16 v[244:247], v[232:235], v[208:215], v1\n\t" \
  "v_smfmac_f32_16x16x64_bf16 v[244:247], v[236:239], v[216:223], v1\n\t" \
  "v_smfmac_f32_16x16x64_bf16 v[248:251], v[232:235], v[192:199], v1\n\t" \
  "v_smfmac_f32_16x16x64_bf16 v[248:251], v[236:239], v[200:207], v1\n\t" \
  "v_smfmac_f32_16x16x64_bf16 v[252:255], v[232:235], v[160:167], v1\n\t" \
  "s_cmp_lt_u32 s64, 2\n\t" \
  "v_smfmac_f32_16x16x64_bf16 v[252:255], v[236:239], v[168:175], v1\n\t" \
  "s_nop 0\n\t" \
  "v_add_f32 v240, v240, v242\n\t" \
  "v_add_f32 v241, v241, v243\n\t" \
  "v_add_f32 v244, v244, v246\n\t" \
  "v_add_f32 v245, v245, v247\n\t" \
  "v_cvt_pk_bf16_f32 v60, v240, v241\n\t" \
  "v_cvt_pk_bf16_f32 v61, v244, v245\n\t" \
  "v_add_f32 v248, v248, v250\n\t" \
  "v_add_f32 v249, v249, v251\n\t" \
  "v_add_f32 v252, v252, v254\n\t" \
  "v_add_f32 v253, v253, v255\n\t" \
  "v_cvt_pk_bf16_f32 v62, v248, v249\n\t" \
  "v_cvt_pk_bf16_f32 v63, v252, v253\n\t" \
  "v_min_u32 v60, -2, v60\n\t" \
  "v_min_u32 v61, -2, v61\n\t" \
  "v_min_u32 v62, -2, v62\n\t" \
  "v_min_u32 v63, -2, v63\n\t" \
  "buffer_store_dwordx4 v[60:63], v10, s[36:39], s81 offen sc1\n\t" \
  "v_and_b32 v52, -4, v2\n\t" \
  "buffer_load_dwordx2 v[52:53], v52, s[36:39], 0 offen sc1\n\t" \
  "s_cbranch_scc0 L_oth%=\n\t" \
  "v_cmp_eq_u32 vcc, 0, v29\n\t" \
  "s_nop 1\n\t" \
  "v_cndmask_b32 v40, 0, v28, vcc\n\t" \
  "v_cmp_eq_u32 vcc, 1, v29\n\t" \
  "s_nop 1\n\t" \
  "v_cndmask_b32 v41, 0, v28, vcc\n\t" \
  "v_cmp_eq_u32 vcc, 2, v29\n\t" \
  "s_nop 1\n\t" \
  "v_cndmask_b32 v42, 0, v28, vcc\n\t" \
  "v_cmp_eq_u32 vcc, 3, v29\n\t" \
  "s_nop 1\n\t" \
  "v_cndmask_b32 v43, 0, v28, vcc\n\t" \
  "ds_read_b128 v[128:131], v9 offset:0\n\t" \
  "ds_read_b128 v[132:135], v9 offset:1024\n\t" \
  "ds_read_b128 v[136:139], v9 offset:2048\n\t" \
  "ds_read_b128 v[140:143], v9 offset:3072\n\t" \
  "ds_read_b128 v[144:147], v9 offset:4096\n\t" \
  "ds_read_b128 v[148:151], v9 offset:5120\n\t" \
  "ds_read_b128 v[152:155], v9 offset:6144\n\t" \
  "ds_read_b128 v[156:159], v9 offset:7168\n\t" \
  "ds_read_b128 v[208:211], v9 offset:8192\n\t" \
  "ds_read_b128 v[212:215], v9 offset:9216\n\t" \
  "ds_read_b128 v[216:219], v9 offset:10240\n\t" \
  "ds_read_b128 v[220:223], v9 offset:11264\n\t" \
  "ds_read_b128 v[192:195], v9 offset:12288\n\t" \
  "ds_read_b128 v[196:199], v9 offset:13312\n\t" \
  "ds_read_b128 v[200:203], v9 offset:14336\n\t" \
  "ds_read_b128 v[204:207], v9 offset:15360\n\t" \
  "s_mov_b32 s93, s67\n\t" \
  "L_nb%=:\n\t" \
  "s_sub_u32 s93, s93, 1\n\t" \
  "s_cbranch_scc0 L_nb%=\n\t" \
  "s_mov_b32 s92, 0\n\t" \
  "s_mov_b32 s24, s82\n\t" \
  "buffer_load_dwordx4 v[224:227], v0, s[36:39], s24 offen offset:0 sc1\n\t" \
  "buffer_load_dwordx4 v[228:231], v0, s[36:39], s24 offen offset:1024 sc1\n\t" \
  "buffer_load_dwordx4 v[232:235], v0, s[36:39], s24 offen offset:2048 sc1\n\t" \
  "buffer_load_dwordx4 v[236:239], v0, s[36:39], s24 offen offset:3072 sc1\n\t" \
  "s_mov_b32 s93, s79\n\t" \
  "L_n20%=:\n\t" \
  "s_sub_u32 s93, s93, 1\n\t" \
  "s_cbranch_scc0 L_n20%=\n\t" \
  "buffer_load_dwordx4 v[160:163], v0, s[36:39], s24 offen offset:0 sc1\n\t" \
  "buffer_load_dwordx4 v[164:167], v0, s[36:39], s24 offen offset:1024 sc1\n\t" \
  "buffer_load_dwordx4 v[168:171], v0, s[36:39], s24 offen offset:2048 sc1\n\t" \
  "buffer_load_dwordx4 v[172:175], v0, s[36:39], s24 offen offset:3072 sc1\n\t" \
  "s_mov_b32 s25, s84\n\t" \
  "s_mov_b32 s26, s86\n\t" \
  "s_add_u32 s60, s36, s85\n\t" \
  "s_addc_u32 s61, s37, 0\n\t" \
  "s_add_u32 s80, s80, 1\n\t" \
  "s_add_u32 s99, s99, 1\n\t" \
  "s_add_u32 s81, s27, s69\n\t" \
  "s_add_u32 s82, s27, s34\n\t" \
  "s_mov_b32 s27, s97\n\t" \
  "s_add_u32 s93, s80, 2\n\t" \
  "s_lshr_b32 s94, s93, 2\n\t" \
  "s_add_u32 s94, s94, 3\n\t" \
  "s_lshr_b32 s94, s66, s94\n\t" \
  "s_and_b32 s94, s94, 1\n\t" \
  "s_lshl_b32 s94, s94, 13\n\t" \
  "s_lshl_b32 s97, s93, 19\n\t" \
  "s_or_b32 s97, s97, s94\n\t" \
  "s_add_u32 s83, s97, s70\n\t" \
  "s_add_u32 s93, s80, 0\n\t" \
  "s_lshr_b32 s94, s93, 2\n\t" \
  "s_lshr_b32 s94, s21, s94\n\t" \
  "s_and_b32 s94, s94, 1\n\t" \
  "s_lshl_b32 s94, s94, 13\n\t" \
  "s_lshl_b32 s89, s93, 19\n\t" \
  "s_or_b32 s89, s89, s94\n\t" \
  "s_add_u32 s89, s89, s74\n\t" \
  "s_xor_b32 s90, s89, 0x40000000\n\t" \
  "s_add_u32 s93, s80, 1\n\t" \
  "s_lshr_b32 s94, s93, 2\n\t" \
  "s_lshr_b32 s94, s23, s94\n\t" \
  "s_and_b32 s94, s94, 1\n\t" \
  "s_lshl_b32 s94, s94, 13\n\t" \
  "s_lshl_b32 s87, s93, 19\n\t" \
  "s_or_b32 s87, s87, s94\n\t" \
  "s_add_u32 s87, s87, s73\n\t" \
  "s_lshr_b32 s94, s93, 2\n\t" \
  "s_add_u32 s94, s94, 16\n\t" \
  "s_lshr_b32 s94, s23, s94\n\t" \
  "s_and_b32 s94, s94, 1\n\t" \
  "s_lshl_b32 s94, s94, 13\n\t" \
  "s_andn2_b32 s88, s87, 0x2000\n\t" \
  "s_or_b32 s88, s88, s94\n\t" \
  "s_add_u32 s88, s88, 33554432\n\t" \
  "s_add_u32 s85, s22, s72\n\t" \
  "s_add_u32 s84, s22, s50\n\t" \
  "s_add_u32 s93, s80, 2\n\t" \
  "s_lshr_b32 s94, s93, 5\n\t" \
  "s_sub_u32 s94, 2, s94\n\t" \
  "s_lshr_b32 s94, s66, s94\n\t" \
  "s_and_b32 s94, s94, 1\n\t" \
  "s_lshl_b32 s94, s94, 13\n\t" \
  "s_lshl_b32 s93, s93, 16\n\t" \
  "s_sub_u32 s22, s94, s93\n\t" \
  "s_add_u32 s86, s22, s65\n\t" \
  "s_cmp_ge_i32 s51, 63\n\t" \
  "s_cbranch_scc1 L_k1e%=\n\t" \
  "s_waitcnt vmcnt(8)\n\t" \
  "s_mov_b32 s35, 0\n\t" \
  "L_k1a%=:\n\t" \
  "v_cmp_eq_u32 vcc, s49, v53\n\t" \
  "v_and_b32 v53, 1, v2\n\t" \
  "s_nop 0\n\t" \
  "v_cndmask_b32 v52, -1, v52, vcc\n\t" \
  "v_and_b32 v53, 1, v2\n\t" \
  "v_lshl_add_u32 v52, v52, 1, v53\n\t" \
  "s_nop 1\n\t" \
  "v_min_i32_dpp v52, v52, v52 row_shr:1 row_mask:0xf bank_mask:0xf\n\t" \
  "s_nop 1\n\t" \
  "v_min_i32_dpp v52, v52, v52 row_shr:2 row_mask:0xf bank_mask:0xf\n\t" \
  "s_nop 1\n\t" \
  "v_min_i32_dpp v52, v52, v52 row_shr:4 row_mask:0xf bank_mask:0xf\n\t" \
  "s_nop 1\n\t" \
  "v_readlane_b32 s51, v52, 7\n\t" \
  "s_nop 1\n\t" \
  "s_add_i32 s51, s51, 1\n\t" \
  "s_add_i32 s93, s80, 1\n\t" \
  "s_min_i32 s93, s93, 63\n\t" \
  "s_cmp_ge_i32 s51, s93\n\t" \
  "s_cbranch_scc1 L_k1e%=\n\t" \
  "s_add_u32 s35, s35, 1\n\t" \
  "s_cmp_lt_u32 s35, s91\n\t" \
  "s_cbranch_scc0 L_k1d%=\n\t" \
  "s_sleep 2\n\t" \
  "v_and_b32 v52, -4, v2\n\t" \
  "buffer_load_dwordx2 v[52:53], v52, s[36:39], 0 offen sc1\n\t" \
  "s_waitcnt vmcnt(0)\n\t" \
  "s_branch L_k1a%=\n\t" \
  "L_k1d%=:\n\t" \
  "s_mov_b32 s91, 1\n\t" \
  "L_k1e%=:\n\t" \
  "s_mov_b32 s35, 0\n\t" \
  "s_mov_b32 m0, s95\n\t" \
  "L_cb%=:\n\t" \
  "s_add_u32 s98, s98, 1\n\t" \
  "s_waitcnt vmcnt(7)\n\t" \
  "v_mfma_f32_16x16x32_bf16 v[44:47], v[40:43], v[224:227], 0\n\t" \
  "v_max3_u32 v30, v224, v225, v226\n\t" \
  "s_waitcnt vmcnt(6)\n\t" \
  "v_mfma_f32_16x16x32_bf16 v[44:47], v[40:43], v[228:231], v[44:47]\n\t" \
  "v_max3_u32 v30, v30, v227, v228\n\t" \
  "v_max3_u32 v31, v229, v230, v231\n\t" \
  "s_waitcnt vmcnt(5)\n\t" \
  "v_mfma_f32_16x16x32_bf16 v[44:47], v[40:43], v[232:235], v[44:47]\n\t" \
  "v_max3_u32 v30, v30, v232, v233\n\t" \
  "v_max3_u32 v31, v31, v234, v235\n\t" \
  "s_waitcnt vmcnt(4)\n\t" \
  "v_mfma_f32_16x16x32_bf16 v[44:47], v[40:43], v[236:239], v[44:47]\n\t" \
  "v_max3_u32 v30, v30, v236, v237\n\t" \
  "v_max3_u32 v31, v31, v238, v239\n\t" \
  "v_max_u32 v30, v30, v31\n\t" \
  "v_cmp_eq_u32 vcc, -1, v30\n\t" \
  "s_cbranch_vccnz L_rb%=\n\t" \
  "L_okb%=:\n\t" \
  "s_nop 2\n\t" \
  "v_add_f32_dpp v48, v44, v44 row_ror:8 row_mask:0xf bank_mask:0xf\n\t" \
  "v_add_f32_dpp v49, v45, v45 row_ror:8 row_mask:0xf bank_mask:0xf\n\t" \
  "v_add_f32_dpp v50, v46, v46 row_ror:8 row_mask:0xf bank_mask:0xf\n\t" \
  "v_add_f32_dpp v51, v47, v47 row_ror:8 row_mask:0xf bank_mask:0xf\n\t" \
  "v_cvt_pk_bf16_f32 v44, v48, v49\n\t" \
  "v_cvt_pk_bf16_f32 v45, v50, v51\n\t" \
  "v_min_u32 v44, -2, v44\n\t" \
  "v_min_u32 v45, -2, v45\n\t" \
  "s_nop 0\n\t" \
  "v_mov_b32_dpp v46, v44 quad_perm:[1,1,3,3] row_mask:0xf bank_mask:0xf\n\t" \
  "v_mov_b32_dpp v47, v45 quad_perm:[1,1,3,3] row_mask:0xf bank_mask:0xf\n\t" \
  "buffer_store_dwordx4 v[44:47], v11, s[36:39], s25 offen sc1\n\t" \
  "L_b0%=:\n\t" \
  "s_waitcnt lgkmcnt(0)\n\t" \
  "s_barrier\n\t" \
  "s_sub_u32 s93, s80, 1\n\t" \
  "v_mov_b32 v30, s93\n\t" \
  "v_mov_b32 v31, s49\n\t" \
  "buffer_store_dwordx2 v[30:31], v10, s[28:31], 0 offen sc1\n\t" \
  "v_mov_b64 v[40:41], 0\n\t" \
  "v_mov_b64 v[42:43], 0\n\t" \
  "v_mov_b64 v[44:45], 0\n\t" \
  "v_mov_b64 v[46:47], 0\n\t" \
  "v_mov_b64 v[48:49], 0\n\t" \
  "v_mov_b64 v[50:51], 0\n\t" \
  "v_mov_b64 v[52:53], 0\n\t" \
  "v_mov_b64 v[54:55], 0\n\t" \
  "v_mov_b64 v[56:57], 0\n\t" \
  "v_mov_b64 v[58:59], 0\n\t" \
  "v_mov_b64 v[60:61], 0\n\t" \
  "v_mov_b64 v[62:63], 0\n\t" \
  "s_mov_b32 s93, s68\n\t" \
  "L_na%=:\n\t" \
  "s_sub_u32 s93, s93, 1\n\t" \
  "s_cbranch_scc0 L_na%=\n\t" \
  "s_waitcnt vmcnt(2) lgkmcnt(0)\n\t" \
  "buffer_load_dwordx4 v[224:227], v0, s[60:63], s52 offen sc1\n\t" \
  "ds_read_b128 v[160:163], v12 offset:0\n\t" \
  "s_nop 4\n\t" \
  "buffer_load_dwordx4 v[228:231], v0, s[60:63], s53 offen sc1\n\t" \
  "ds_read_b128 v[164:167], v12 offset:1024\n\t" \
  "s_nop 4\n\t" \
  "buffer_load_dwordx4 v[232:235], v0, s[60:63], s54 offen sc1\n\t" \
  "ds_read_b128 v[168:171], v12 offset:2048\n\t" \
  "s_nop 4\n\t" \
  "buffer_load_dwordx4 v[236:239], v0, s[60:63], s55 offen sc1\n\t" \
  "ds_read_b128 v[172:175], v12 offset:3072\n\t" \
  "s_nop 4\n\t" \
  "buffer_load_dwordx4 v[240:243], v0, s[60:63], s56 offen sc1\n\t" \
  "ds_read_b128 v[176:179], v12 offset:4096\n\t" \
  "s_nop 4\n\t" \
  "buffer_load_dwordx4 v[244:247], v0, s[60:63], s57 offen sc1\n\t" \
  "ds_read_b128 v[180:183], v12 offset:5120\n\t" \
  "s_nop 4\n\t" \
  "buffer_load_dwordx4 v[248:251], v0, s[60:63], s58 offen sc1\n\t" \
  "ds_read_b128 v[184:187], v12 offset:6144\n\t" \
  "s_nop 4\n\t" \
  "buffer_load_dwordx4 v[252:255], v0, s[60:63], s59 offen sc1\n\t" \
  "ds_read_b128 v[188:191], v12 offset:7168\n\t" \
  "s_branch L_top%=\n\t" \
  "L_oth%=:\n\t" \
  "ds_read_b128 v[128:131], v9 offset:0\n\t" \
  "ds_read_b128 v[132:135], v9 offset:1024\n\t" \
  "ds_read_b128 v[136:139], v9 offset:2048\n\t" \
  "ds_read_b128 v[140:143], v9 offset:3072\n\t" \
  "ds_read_b128 v[144:147], v9 offset:4096\n\t" \
  "ds_read_b128 v[148:151], v9 offset:5120\n\t" \
  "ds_read_b128 v[152:155], v9 offset:6144\n\t" \
  "ds_read_b128 v[156:159], v9 offset:7168\n\t" \
  "ds_read_b128 v[208:211], v9 offset:8192\n\t" \
  "ds_read_b128 v[212:215], v9 offset:9216\n\t" \
  "ds_read_b128 v[216:219], v9 offset:10240\n\t" \
  "ds_read_b128 v[220:223], v9 offset:11264\n\t" \
  "ds_read_b128 v[192:195], v9 offset:12288\n\t" \
  "ds_read_b128 v[196:199], v9 offset:13312\n\t" \
  "ds_read_b128 v[200:203], v9 offset:14336\n\t" \
  "ds_read_b128 v[204:207], v9 offset:15360\n\t" \
  "s_mov_b32 s25, s84\n\t" \
  "s_mov_b32 s26, s86\n\t" \
  "s_add_u32 s60, s36, s85\n\t" \
  "s_addc_u32 s61, s37, 0\n\t" \
  "s_add_u32 s80, s80, 1\n\t" \
  "s_add_u32 s99, s99, 1\n\t" \
  "s_add_u32 s81, s27, s69\n\t" \
  "s_add_u32 s82, s27, s34\n\t" \
  "s_mov_b32 s27, s97\n\t" \
  "s_add_u32 s93, s80, 2\n\t" \
  "s_lshr_b32 s94, s93, 2\n\t" \
  "s_add_u32 s94, s94, 3\n\t" \
  "s_lshr_b32 s94, s66, s94\n\t" \
  "s_and_b32 s94, s94, 1\n\t" \
  "s_lshl_b32 s94, s94, 13\n\t" \
  "s_lshl_b32 s97, s93, 19\n\t" \
  "s_or_b32 s97, s97, s94\n\t" \
  "s_add_u32 s83, s97, s70\n\t" \
  "s_add_u32 s93, s80, 0\n\t" \
  "s_lshr_b32 s94, s93, 2\n\t" \
  "s_lshr_b32 s94, s21, s94\n\t" \
  "s_and_b32 s94, s94, 1\n\t" \
  "s_lshl_b32 s94, s94, 13\n\t" \
  "s_lshl_b32 s89, s93, 19\n\t" \
  "s_or_b32 s89, s89, s94\n\t" \
  "s_add_u32 s89, s89, s74\n\t" \
  "s_xor_b32 s90, s89, 0x40000000\n\t" \
  "s_add_u32 s93, s80, 1\n\t" \
  "s_lshr_b32 s94, s93, 2\n\t" \
  "s_lshr_b32 s94, s23, s94\n\t" \
  "s_and_b32 s94, s94, 1\n\t" \
  "s_lshl_b32 s94, s94, 13\n\t" \
  "s_lshl_b32 s87, s93, 19\n\t" \
  "s_or_b32 s87, s87, s94\n\t" \
  "s_add_u32 s87, s87, s73\n\t" \
  "s_lshr_b32 s94, s93, 2\n\t" \
  "s_add_u32 s94, s94, 16\n\t" \
  "s_lshr_b32 s94, s23, s94\n\t" \
  "s_and_b32 s94, s94, 1\n\t" \
  "s_lshl_b32 s94, s94, 13\n\t" \
  "s_andn2_b32 s88, s87, 0x2000\n\t" \
  "s_or_b32 s88, s88, s94\n\t" \
  "s_add_u32 s88, s88, 33554432\n\t" \
  "s_add_u32 s85, s22, s72\n\t" \
  "s_add_u32 s84, s22, s50\n\t" \
  "s_add_u32 s93, s80, 2\n\t" \
  "s_lshr_b32 s94, s93, 5\n\t" \
  "s_sub_u32 s94, 2, s94\n\t" \
  "s_lshr_b32 s94, s66, s94\n\t" \
  "s_and_b32 s94, s94, 1\n\t" \
  "s_lshl_b32 s94, s94, 13\n\t" \
  "s_lshl_b32 s93, s93, 16\n\t" \
  "s_sub_u32 s22, s94, s93\n\t" \
  "s_add_u32 s86, s22, s65\n\t" \
  "s_cmp_ge_i32 s51, 63\n\t" \
  "s_cbranch_scc1 L_k2e%=\n\t" \
  "s_waitcnt vmcnt(0)\n\t" \
  "s_mov_b32 s35, 0\n\t" \
  "L_k2a%=:\n\t" \
  "v_cmp_eq_u32 vcc, s49, v53\n\t" \
  "v_and_b32 v53, 1, v2\n\t" \
  "s_nop 0\n\t" \
  "v_cndmask_b32 v52, -1, v52, vcc\n\t" \
  "v_and_b32 v53, 1, v2\n\t" \
  "v_lshl_add_u32 v52, v52, 1, v53\n\t" \
  "s_nop 1\n\t" \
  "v_min_i32_dpp v52, v52, v52 row_shr:1 row_mask:0xf bank_mask:0xf\n\t" \
  "s_nop 1\n\t" \
  "v_min_i32_dpp v52, v52, v52 row_shr:2 row_mask:0xf bank_mask:0xf\n\t" \
  "s_nop 1\n\t" \
  "v_min_i32_dpp v52, v52, v52 row_shr:4 row_mask:0xf bank_mask:0xf\n\t" \
  "s_nop 1\n\t" \
  "v_readlane_b32 s51, v52, 7\n\t" \
  "s_nop 1\n\t" \
  "s_add_i32 s51, s51, 1\n\t" \
  "s_add_i32 s93, s80, 1\n\t" \
  "s_min_i32 s93, s93, 63\n\t" \
  "s_cmp_ge_i32 s51, s93\n\t" \
  "s_cbranch_scc1 L_k2e%=\n\t" \
  "s_add_u32 s35, s35, 1\n\t" \
  "s_cmp_lt_u32 s35, s91\n\t" \
  "s_cbranch_scc0 L_k2d%=\n\t" \
  "s_sleep 2\n\t" \
  "v_and_b32 v52, -4, v2\n\t" \
  "buffer_load_dwordx2 v[52:53], v52, s[36:39], 0 offen sc1\n\t" \
  "s_waitcnt vmcnt(0)\n\t" \
  "s_branch L_k2a%=\n\t" \
  "L_k2d%=:\n\t" \
  "s_mov_b32 s91, 1\n\t" \
  "L_k2e%=:\n\t" \
  "s_mov_b32 s35, 0\n\t" \
  "s_mov_b32 m0, s95\n\t" \
  "v_mov_b32 v44, -1\n\t" \
  "v_mov_b32 v45, -1\n\t" \
  "v_mov_b32 v46, -1\n\t" \
  "v_mov_b32 v47, -1\n\t" \
  "buffer_store_dwordx4 v[44:47], v11, s[36:39], s26 offen sc1\n\t" \
  "s_waitcnt vmcnt(0)\n\t" \
  "s_branch L_b0%=\n\t" \
  "L_rb%=:\n\t" \
  "L_rb2%=:\n\t" \
  "s_add_u32 s92, s92, 1\n\t" \
  "s_cmp_lt_u32 s92, s91\n\t" \
  "s_cbranch_scc0 L_db%=\n\t" \
  "s_nop 4\n\t" \
  "buffer_load_dwordx4 v[224:227], v0, s[36:39], s24 offen offset:0 sc1\n\t" \
  "buffer_load_dwordx4 v[228:231], v0, s[36:39], s24 offen offset:1024 sc1\n\t" \
  "buffer_load_dwordx4 v[232:235], v0, s[36:39], s24 offen offset:2048 sc1\n\t" \
  "buffer_load_dwordx4 v[236:239], v0, s[36:39], s24 offen offset:3072 sc1\n\t" \
  "s_add_u32 s98, s98, 1\n\t" \
  "s_waitcnt vmcnt(7)\n\t" \
  "v_mfma_f32_16x16x32_bf16 v[44:47], v[40:43], v[160:163], 0\n\t" \
  "v_max3_u32 v30, v160, v161, v162\n\t" \
  "s_waitcnt vmcnt(6)\n\t" \
  "v_mfma_f32_16x16x32_bf16 v[44:47], v[40:43], v[164:167], v[44:47]\n\t" \
  "v_max3_u32 v30, v30, v163, v164\n\t" \
  "v_max3_u32 v31, v165, v166, v167\n\t" \
  "s_waitcnt vmcnt(5)\n\t" \
  "v_mfma_f32_16x16x32_bf16 v[44:47], v[40:43], v[168:171], v[44:47]\n\t" \
  "v_max3_u32 v30, v30, v168, v169\n\t" \
  "v_max3_u32 v31, v31, v170, v171\n\t" \
  "s_waitcnt vmcnt(4)\n\t" \
  "v_mfma_f32_16x16x32_bf16 v[44:47], v[40:43], v[172:175], v[44:47]\n\t" \
  "v_max3_u32 v30, v30, v172, v173\n\t" \
  "v_max3_u32 v31, v31, v174, v175\n\t" \
  "v_max_u32 v30, v30, v31\n\t" \
  "v_cmp_eq_u32 vcc, -1, v30\n\t" \
  "s_cbranch_vccz L_okb%=\n\t" \
  "s_nop 4\n\t" \
  "buffer_load_dwordx4 v[160:163], v0, s[36:39], s24 offen offset:0 sc1\n\t" \
  "buffer_load_dwordx4 v[164:167], v0, s[36:39], s24 offen offset:1024 sc1\n\t" \
  "buffer_load_dwordx4 v[168:171], v0, s[36:39], s24 offen offset:2048 sc1\n\t" \
  "buffer_load_dwordx4 v[172:175], v0, s[36:39], s24 offen offset:3072 sc1\n\t" \
  "s_add_u32 s98, s98, 1\n\t" \
  "s_waitcnt vmcnt(7)\n\t" \
  "v_mfma_f32_16x16x32_bf16 v[44:47], v[40:43], v[224:227], 0\n\t" \
  "v_max3_u32 v30, v224, v225, v226\n\t" \
  "s_waitcnt vmcnt(6)\n\t" \
  "v_mfma_f32_16x16x32_bf16 v[44:47], v[40:43], v[228:231], v[44:47]\n\t" \
  "v_max3_u32 v30, v30, v227, v228\n\t" \
  "v_max3_u32 v31, v229, v230, v231\n\t" \
  "s_waitcnt vmcnt(5)\n\t" \
  "v_mfma_f32_16x16x32_bf16 v[44:47], v[40:43], v[232:235], v[44:47]\n\t" \
  "v_max3_u32 v30, v30, v232, v233\n\t" \
  "v_max3_u32 v31, v31, v234, v235\n\t" \
  "s_waitcnt vmcnt(4)\n\t" \
  "v_mfma_f32_16x16x32_bf16 v[44:47], v[40:43], v[236:239], v[44:47]\n\t" \
  "v_max3_u32 v30, v30, v236, v237\n\t" \
  "v_max3_u32 v31, v31, v238, v239\n\t" \
  "v_max_u32 v30, v30, v31\n\t" \
  "v_cmp_eq_u32 vcc, -1, v30\n\t" \
  "s_cbranch_vccz L_okb%=\n\t" \
  "s_branch L_rb2%=\n\t" \
  "L_db%=:\n\t" \
  "s_mov_b32 s91, 1\n\t" \
  "s_branch L_okb%=\n\t" \
  "L_ra0%=:\n\t" \
  "s_add_u32 s35, s35, 1\n\t" \
  "s_cmp_lt_u32 s35, s91\n\t" \
  "s_cbranch_scc0 L_da0%=\n\t" \
  "buffer_load_dwordx4 v[224:227], v0, s[60:63], s52 offen sc1\n\t" \
  "buffer_load_dwordx4 v[228:231], v0, s[60:63], s53 offen sc1\n\t" \
  "s_add_u32 s99, s99, 1\n\t" \
  "s_waitcnt vmcnt(0)\n\t" \
  "v_max3_u32 v30, v224, v225, v226\n\t" \
  "v_max_u32 v30, v30, v227\n\t" \
  "v_max3_u32 v30, v30, v228, v229\n\t" \
  "v_max3_u32 v30, v30, v230, v231\n\t" \
  "v_cmp_eq_u32 vcc, -1, v30\n\t" \
  "s_cbranch_vccnz L_ra0%=\n\t" \
  "s_branch L_ok0%=\n\t" \
  "L_da0%=:\n\t" \
  "s_mov_b32 s91, 1\n\t" \
  "s_branch L_ok0%=\n\t" \
  "L_ra1%=:\n\t" \
  "s_add_u32 s35, s35, 1\n\t" \
  "s_cmp_lt_u32 s35, s91\n\t" \
  "s_cbranch_scc0 L_da1%=\n\t" \
  "buffer_load_dwordx4 v[232:235], v0, s[60:63], s54 offen sc1\n\t" \
  "buffer_load_dwordx4 v[236:239], v0, s[60:63], s55 offen sc1\n\t" \
  "s_add_u32 s99, s99, 1\n\t" \
  "s_waitcnt vmcnt(0)\n\t" \
  "v_max3_u32 v30, v232, v233, v234\n\t" \
  "v_max_u32 v30, v30, v235\n\t" \
  "v_max3_u32 v30, v30, v236, v237\n\t" \
  "v_max3_u32 v30, v30, v238, v239\n\t" \
  "v_cmp_eq_u32 vcc, -1, v30\n\t" \
  "s_cbranch_vccnz L_ra1%=\n\t" \
  "s_branch L_ok1%=\n\t" \
  "L_da1%=:\n\t" \
  "s_mov_b32 s91, 1\n\t" \
  "s_branch L_ok1%=\n\t" \
  "L_ra2%=:\n\t" \
  "s_add_u32 s35, s35, 1\n\t" \
  "s_cmp_lt_u32 s35, s91\n\t" \
  "s_cbranch_scc0 L_da2%=\n\t" \
  "buffer_load_dwordx4 v[240:243], v0, s[60:63], s56 offen sc1\n\t" \
  "buffer_load_dwordx4 v[244:247], v0, s[60:63], s57 offen sc1\n\t" \
  "s_add_u32 s99, s99, 1\n\t" \
  "s_waitcnt vmcnt(0)\n\t" \
  "v_max3_u32 v30, v240, v241, v242\n\t" \
  "v_max_u32 v30, v30, v243\n\t" \
  "v_max3_u32 v30, v30, v244, v245\n\t" \
  "v_max3_u32 v30, v30, v246, v247\n\t" \
  "v_cmp_eq_u32 vcc, -1, v30\n\t" \
  "s_cbranch_vccnz L_ra2%=\n\t" \
  "s_branch L_ok2%=\n\t" \
  "L_da2%=:\n\t" \
  "s_mov_b32 s91, 1\n\t" \
  "s_branch L_ok2%=\n\t" \
  "L_ra3%=:\n\t" \
  "s_add_u32 s35, s35, 1\n\t" \
  "s_cmp_lt_u32 s35, s91\n\t" \
  "s_cbranch_scc0 L_da3%=\n\t" \
  "buffer_load_dwordx4 v[248:251], v0, s[60:63], s58 offen sc1\n\t" \
  "buffer_load_dwordx4 v[252:255], v0, s[60:63], s59 offen sc1\n\t" \
  "s_add_u32 s99, s99, 1\n\t" \
  "s_waitcnt vmcnt(0)\n\t" \
  "v_max3_u32 v30, v248, v249, v250\n\t" \
  "v_max_u32 v30, v30, v251\n\t" \
  "v_max3_u32 v30, v30, v252, v253\n\t" \
  "v_max3_u32 v30, v30, v254, v255\n\t" \
  "v_cmp_eq_u32 vcc, -1, v30\n\t" \
  "s_cbranch_vccnz L_ra3%=\n\t" \
  "s_branch L_ok3%=\n\t" \
  "L_da3%=:\n\t" \
  "s_mov_b32 s91, 1\n\t" \
  "s_branch L_ok3%=\n\t" \
  "L_done%=:\n\t" \
  "s_waitcnt vmcnt(0) lgkmcnt(0)\n\t" \
  "s_barrier\n\t" \
  "v_mov_b32 v30, 63\n\t" \
  "v_mov_b32 v31, s49\n\t" \
  "buffer_store_dwordx2 v[30:31], v10, s[28:31], 0 offen sc1\n\t" \
  "s_cmp_eq_u32 s91, 1\n\t" \
  "s_cselect_b32 s93, 0, 0x40000000\n\t" \
  "s_add_u32 s93, s93, 6226176\n\t" \
  "v_mov_b32 v40, 1\n\t" \
  "v_mov_b32 v41, s49\n\t" \
  "buffer_store_dwordx2 v[40:41], off, s[36:39], s93 sc1\n\t" \
  "s_waitcnt lgkmcnt(0)\n\t"
#define CHAIN_CLOBBERS "v16", "v17", "v18", "v19", "v20", "v21", "v22", "v23", "v24", "v25", "v26", "v27", "v30", "v31", "v32", "v33", "v34", "v35", "v36", "v37", "v38", "v39", "v41", "v42", "v43", "v44", "v45", "v46", "v47", "v48", "v49", "v50", "v51", "v52", "v53", "v54", "v55", "v56", "v57", "v58", "v59", "v60", "v61", "v62", "v63", "v64", "v65", "v66", "v67", "v68", "v69", "v70", "v71", "v72", "v73", "v74", "v75", "v76", "v77", "v78", "v79", "v80", "v81", "v82", "v83", "v84", "v85", "v86", "v87", "v88", "v89", "v90", "v91", "v92", "v93", "v94", "v95", "v96", "v97", "v98", "v99", "v100", "v101", "v102", "v103", "v104", "v105", "v106", "v107", "v108", "v109", "v110", "v111", "v112", "v113", "v114", "v115", "v116", "v117", "v118", "v119", "v120", "v121", "v122", "v123", "v124", "v125", "v126", "v127", "v128", "v129", "v130", "v131", "v132", "v133", "v134", "v135", "v136", "v137", "v138", "v139", "v140", "v141", "v142", "v143", "v144", "v145", "v146", "v147", "v148", "v149", "v150", "v151", "v152", "v153", "v154", "v155", "v156", "v157", "v158", "v159", "v160", "v161", "v162", "v163", "v164", "v165", "v166", "v167", "v168", "v169", "v170", "v171", "v172", "v173", "v174", "v175", "v176", "v177", "v178", "v179", "v180", "v181", "v182", "v183", "v184", "v185", "v186", "v187", "v188", "v189", "v190", "v191", "v192", "v193", "v194", "v195", "v196", "v197", "v198", "v199", "v200", "v201", "v202", "v203", "v204", "v205", "v206", "v207", "v208", "v209", "v210", "v211", "v212", "v213", "v214", "v215", "v216", "v217", "v218", "v219", "v220", "v221", "v222", "v223", "v240", "v241", "v242", "v243", "v244", "v245", "v246", "v247", "v248", "v249", "v250", "v251", "v252", "v253", "v254", "v255", "a0", "a1", "a2", "a3", "a4", "a5", "a6", "a7", "a8", "a9", "a10", "a11", "a12", "a13", "a14", "a15", "a16", "a17", "a18", "a19", "a20", "a21", "a22", "a23", "a24", "a25", "a26", "a27", "a28", "a29", "a30", "a31", "a32", "a33", "a34", "a35", "a36", "a37", "a38", "a39", "a40", "a41", "a42", "a43", "a44", "a45", "a46", "a47", "a48", "a49", "a50", "a51", "a52", "a53", "a54", "a55", "a56", "a57", "a58", "a59", "a60", "a61", "a62", "a63", "a64", "a65", "a66", "a67", "a68", "a69", "a70", "a71", "a72", "a73", "a74", "a75", "a76", "a77", "a78", "a79", "a80", "a81", "a82", "a83", "a84", "a85", "a86", "a87", "a88", "a89", "a90", "a91", "a92", "a93", "a94", "a95", "a96", "a97", "a98", "a99", "a100", "a101", "a102", "a103", "a104", "a105", "a106", "a107", "a108", "a109", "a110", "a111", "a112", "a113", "a114", "a115", "a116", "a117", "a118", "a119", "a120", "a121", "a122", "a123", "a124", "a125", "a126", "a127", "a128", "a129", "a130", "a131", "a132", "a133", "a134", "a135", "a136", "a137", "a138", "a139", "a140", "a141", "a142", "a143", "a144", "a145", "a146", "a147", "a148", "a149", "a150", "a151", "a152", "a153", "a154", "a155", "a156", "a157", "a158", "a159", "a160", "a161", "a162", "a163", "a164", "a165", "a166", "a167", "a168", "a169", "a170", "a171", "a172", "a173", "a174", "a175", "a176", "a177", "a178", "a179", "a180", "a181", "a182", "a183", "a184", "a185", "a186", "a187", "a188", "a189", "a190", "a191", "a192", "a193", "a194", "a195", "a196", "a197", "a198", "a199", "a200", "a201", "a202", "a203", "a204", "a205", "a206", "a207", "a208", "a209", "a210", "a211", "a212", "a213", "a214", "a215", "a216", "a217", "a218", "a219", "a220", "a221", "a222", "a223", "a224", "a225", "a226", "a227", "a228", "a229", "a230", "a231", "a232", "a233", "a234", "a235", "a236", "a237", "a238", "a239", "a240", "a241", "a242", "a243", "a244", "a245", "a246", "a247", "a248", "a249", "a250", "a251", "a252", "a253", "a254", "a255", "s80", "s81", "s82", "s83", "s84", "s85", "s86", "s87", "s88", "s89", "s90", "s91", "s92", "s93", "s94", "s95", "s96", "s97", "s21", "s22", "s23", "s24", "s25", "s26", "s27", "s34", "s35", "vcc", "scc", "memory"
constexpr int L_WOI = 0, L_WVI = 65536, L_MEM = 131072, L_QK = 139264, L_OI = 143360, L_KB = 145408, L_VB = 153600, L_DUMP = 161792;
__device__ __forceinline__ void chain_wg(const Params& p, const int h, const int cs, const int rep, const int rot, const int w, const unsigned ep, LAS char* lds) {
  const int lane = lane_id(), tid = w * 64 + lane, g = lane >> 4, c = lane & 15;
  char* const ws = p.ws;
  const int q4 = c >> 2, p4 = c & 3, tok = 2 * (c >> 2) + (c & 1), hf = (c >> 1) & 1, rho = 32 * cs + h;
  u32x16 vp;
  unsigned vm0 = L_MEM + tok * 1056 + (2 * g + hf) * 4;
  unsigned sc[16];
  unsigned vc[15];
  vc[0] = lane * 16;
  vc[1] = (c & 2) ? 0xEEEE : 0x4444;
  vc[2] = (unsigned)C_CTL + (CTL_E1 + (((lane >> 2) & 1) * 32 + h) * 16 + (lane & 3) * 4) * 4 + ((lane >> 1) & 1);
  vc[3] = L_QK + (32 * w + c) * 32 + ((g ^ q4) * 8);
  vc[4] = L_QK + (4 * g + q4) * 32 + ((p4 ^ g) * 8);
  vc[5] = (c & 2) ? L_OI + tok * 256 + (((4 * w + g) ^ (tok << 1)) & 15) * 16 : L_DUMP + (w * 32 + g * 8 + tok) * 16;
  vc[6] = L_OI + tok * 256 + (((2 * g + hf) ^ (tok << 1)) & 15) * 16;
  vc[7] = L_OI + tok * 256 + (((8 + 2 * g + hf) ^ (tok << 1)) & 15) * 16;
  vc[8] = L_WOI + w * 16384 + lane * 16;
  vc[9] = L_WVI + w * 16384 + lane * 16;
  vc[10] = (c >> 2) * 16384 + (g >> 1) * 4096 + (4 * (g & 1) + (c & 3)) * 16;
  vc[11] = (3 - (2 * (g >> 1) + (c >> 3))) * 16384 + (4 * ((c >> 2) & 1) + (c & 2) + (g & 1)) * 16 + (c & 1) * 0x20000000;
  vc[12] = L_KB + lane * 16;
  vc[14] = L_VB + w * 2048 + lane * 16;
  vc[13] = vc[4] + w * 1024;
  #pragma unroll
  for (int n = 0; n < 4; ++n) {
    const int row = 16 * w + 4 * n + g;
    const int fq = (row & 3) | (((row >> 4) & 1) << 2);
    const int fo = (row & 1) | (((row >> 2) & 1) << 1) | (((row >> 4) & 1) << 2);
    vp[n] = row * 8192 + h * 256 + ((c ^ (2 * fq)) * 16);
    vp[4 + n] = (h * 128 + row) * 1024 + cs * 512 + ((c ^ (2 * fo)) * 16);
  }
  {
    const int rb = 4 * (g >> 1) + (g & 1) + 16 * (q4 & 1) + 2 * (q4 >> 1);
    const int fr = (rb & 3) | (((rb >> 4) & 1) << 2);
    #pragma unroll
    for (int t = 0; t < 2; ++t) vp[8 + t] = rb * 256 + (((2 * (2 * w + t) + (p4 >> 1)) ^ (2 * fr)) * 16) + (p4 & 1) * 8;
    const int ro = 16 * (g & 1) + 8 * (g >> 1) + (q4 & 1) + 4 * (q4 >> 1);
    const int fo = (ro & 1) | (((ro >> 2) & 1) << 1) | (((ro >> 4) & 1) << 2);
    #pragma unroll
    for (int t = 0; t < 4; ++t) vp[10 + t] = (w >> 1) * 16384 + ro * 256 + (((2 * (4 * (w & 1) + t) + (p4 >> 1)) ^ (2 * fo)) * 16) + (p4 & 1) * 8;
    vp[14] = 0; vp[15] = 0;
  }
  const u32x2 sel = {0x3f80u << (16 * ((c >> 2) & 1)), (unsigned)(c & 3)};
  const float c1 = 0.08838834764831845f * 1.4426950408889634f;
  sc[0] = w; sc[1] = cs; sc[2] = ep; sc[3] = p.dB & 0xffff; sc[4] = p.dA;
  sc[5] = (16 * cs + 4 * w) * 16384 + h * 128;
  sc[6] = (rho >> 1) * 16384 + (rho & 1) * 4096;
  sc[7] = rho * 128;
  sc[8] = (3 - rep) * 16384 + rot;
  sc[9] = h * 16384 + w * 2048;
  sc[10] = h * 16384 + cs * (4096 + 0x40000000) + w * 1024;
  sc[11] = w * 4096;
  sc[12] = __float_as_uint(c1); sc[13] = __float_as_uint(c1); sc[14] = __float_as_uint(-c1); sc[15] = p.dB >> 16;
  const u32x4 r_comm = mk_rsrc(ws, (unsigned)C_END), r_kx = mk_rsrc(ws + W_KX, 64u << 20), r_km = mk_rsrc(ws + W_KM, 32u << 20);
  u32x4 rq = mk_rsrc(p.awq, DM * 4096 * 2), rk = mk_rsrc(p.awk, DM * 4096 * 2), rv = mk_rsrc(p.awv, DM * 4096 * 2), ro = mk_rsrc(p.awo, DM * 4096 * 2);
  const u32x8 vca = {vc[0], vc[1], vc[2], vc[3], vc[4], vc[5], vc[6], vc[7]};
  const u32x4 vcb = {vc[8], vc[9], vc[10], vc[11]};
  const u32x3 vcc = {vc[12], vc[13], vc[14]};
  u32x4 sa = {sc[0], sc[1], sc[2], sc[3]}, sb = {sc[4], sc[5], sc[6], sc[7]}, sd = {sc[8], sc[9], sc[10], sc[11]}, se = {sc[12], sc[13], sc[14], sc[15]};
  u32x4 rm0 = mk_rsrc(p.amem0, 8 * DM * 2);
  unsigned np_b, np_a;
  asm volatile(CHAIN_ASM
    : "+{v[224:239]}"(vp), "+{v40}"(vm0), "+{s[28:31]}"(rm0), "+{s[48:51]}"(rq), "+{s[52:55]}"(rk), "+{s[56:59]}"(rv), "+{s[60:63]}"(ro), "+{s[64:67]}"(sa), "+{s[68:71]}"(sb), "+{s[72:75]}"(sd), "+{s[76:79]}"(se), "={s98}"(np_b), "={s99}"(np_a)
    : "{v[0:7]}"(vca), "{v[8:11]}"(vcb), "{v[12:14]}"(vcc), "{v[28:29]}"(sel), "{s[36:39]}"(r_comm), "{s[40:43]}"(r_kx), "{s[44:47]}"(r_km)
    : CHAIN_CLOBBERS);
}

__global__ void __launch_bounds__(256, 1) fused_kernel(Params) {
  extern __shared__ __attribute__((aligned(16))) char lds_raw[];
  LAS char* lds = (LAS char*)lds_raw;
  const int b = blockIdx.x, i = b >> 3;
  int x; unsigned ep;
  {
    int* const cw = (int*)(args().ws + C_CTL);
    ep = threadIdx.x < 64 ? __builtin_amdgcn_readfirstlane(__hip_atomic_load((const GAS unsigned*)cw + CTL_EPOCH, __ATOMIC_RELAXED, __HIP_MEMORY_SCOPE_AGENT)) + 1u : 0u;
    const Ctl ctl{cw, ep};
    if (threadIdx.x < 64) {
      if (b == 0 && threadIdx.x < 8) ctl_st(ctl, CTL_ROT + 32 * threadIdx.x, (int)(__builtin_amdgcn_s_getreg(20 | (3 << 11)) & 7u));
      if (threadIdx.x == 0) ctl_st(ctl, CTL_STA + 4 * b, 0);
      int r0; unsigned sp = 0;
      while ((r0 = __builtin_amdgcn_readfirstlane(ctl_ld(ctl, CTL_ROT + 32 * (b & 7)))) < 0) { if (++sp > (SPIN_CAP << 4)) { ctl_st(ctl, CTL_TMO, 1); r0 = 0; break; } }
      *(LAS int*)(lds + 163832) = r0; *(LAS unsigned*)(lds + 163836) = ep;
    }
    lds_barrier();
    const int r = __builtin_amdgcn_readfirstlane(*(LAS int*)(lds + 163832));
    ep = __builtin_amdgcn_readfirstlane(*(LAS unsigned*)(lds + 163836));
    x = (b + r) & 7;
  }
  const int w = __builtin_amdgcn_readfirstlane(threadIdx.x >> 6);
  if (x >= 4) engine_wg((x - 4) * 32 + i, w, ep, lds);
  if (i >= 16) {
    const Params p = args();
    prefetch_weights(p, x * 8 + ((i - 16) >> 1), i & 1, (i - 16) >> 1, w, lds);
    const Ctl ctl{(int*)(p.ws + C_CTL), ep};
    if (i == 31 && x == 0) {
      wait_ge(ctl, CTL_STA + 4 * (w * 64 + lane_id()), true, 0);
      lds_barrier();
      if (w == 0) __hip_atomic_store((GAS unsigned*)ctl.w + CTL_EPOCH, ep, __ATOMIC_RELAXED, __HIP_MEMORY_SCOPE_AGENT);
    }
    if (i >= 24) WG_END();
    const int hd = x * 8 + (i - 16);
    attn_b_wg(p, ctl, hd, 0, 4, 64 + hd, lds, w, lane_id());
    WG_END();
  }
  chain_wg(args(), x * 8 + (i >> 1), i & 1, x, i >> 1, w, ep, lds);
  WG_END();
}

constexpr int g_dB = 8 | (4 << 16), g_dA = 17;
extern "C" void kernel_launch(void* const* d_in, const int* in_sizes, int n_in, void* d_out, int out_size, void* d_ws, size_t ws_size, hipStream_t stream) {
  Params p;
  p.ax = (const u16*)d_in[0]; p.amem0 = (const u16*)d_in[1]; p.awq = (const u16*)d_in[2]; p.awk = (const u16*)d_in[3]; p.awv = (const u16*)d_in[4]; p.awo = (const u16*)d_in[5];
  p.bx = (const u16*)d_in[7]; p.bwqkv = (const u16*)d_in[8]; p.bwo = (const u16*)d_in[9];
  p.out = (u16*)d_out; p.ws = (char*)(((uintptr_t)d_ws + PAGE - 1) & ~(uintptr_t)(PAGE - 1)); p.dB = g_dB; p.dA = g_dA;
  (void)hipFuncSetAttribute((const void*)fused_kernel, hipFuncAttributeMaxDynamicSharedMemorySize, LDS_BYTES);
  fused_kernel<<<256, 256, LDS_BYTES, stream>>>(p);
}
```
